```python
import torch

def _softmax(x, axis=-1):
    return torch.softmax(x, dim=axis)
def _gelu(x):
    return torch.nn.functional.gelu(x, approximate="tanh")
def _silu(x):
    return torch.nn.functional.silu(x)
def _sigmoid(x):
    return torch.sigmoid(x)
def _layernorm(x, axis=-1, eps=1e-5):
    m = x.mean(dim=axis, keepdim=True)
    v = x.var(dim=axis, keepdim=True, unbiased=False)
    return (x - m) / torch.sqrt(v + eps)
def _scatter_add(tgt, idx, src, axis=0):
    return tgt.clone().index_add_(axis, idx, src)
def _segment_sum(src, seg, n):
    out = torch.zeros((n,) + src.shape[1:], dtype=src.dtype)
    return out.index_add_(0, seg, src)
def _pad(x, pw):
    flat = tuple(v for pair in reversed(pw) for v in pair)
    return torch.nn.functional.pad(x, flat)

kernel_name = 'sequential_preLN__mha_block__geglu_ffn__prompt'

def setup_inputs():

    _w = (lambda a: (lambda t: t.to(torch.bfloat16) if t.is_floating_point() else t)(torch.as_tensor(a)))
    def _A_setup():
        B, S, D, H = 1, 2048, 576, 32
        return {
            "x": _w((lambda *s: torch.randn(*s, dtype=torch.bfloat16) * 0.05)(B, S, D)),
            "wqkv": _w((lambda *s: torch.randn(*s, dtype=torch.bfloat16) * 0.05)(D, 3 * D)),
            "wo": _w((lambda *s: torch.randn(*s, dtype=torch.bfloat16) * 0.05)(D, D)),
        }
    _A_in = _A_setup()

    def _B_setup():
        B, S, D, F = 1, 2048, 576, 4608
        return {
            "x": _w((lambda *s: torch.randn(*s, dtype=torch.bfloat16) * 0.05)(B, S, D)),
            "w_gate": _w((lambda *s: torch.randn(*s, dtype=torch.bfloat16) * 0.05)(D, F)),
            "w_up": _w((lambda *s: torch.randn(*s, dtype=torch.bfloat16) * 0.05)(D, F)),
            "w_down": _w((lambda *s: torch.randn(*s, dtype=torch.bfloat16) * 0.05)(F, D)),
        }
    _B_in = _B_setup()

    return {
        "A_x": _A_in["x"],
        "A_wqkv": _A_in["wqkv"],
        "A_wo": _A_in["wo"],
        "B_x": _B_in["x"],
        "B_w_gate": _B_in["w_gate"],
        "B_w_up": _B_in["w_up"],
        "B_w_down": _B_in["w_down"],
    }

def reference(A_x, A_wqkv, A_wo, B_x, B_w_gate, B_w_up, B_w_down):
    def _A_ref(x, wqkv, wo):
        B, S, D = x.shape
        H = 32
        Dh = D // H
        qkv = (lambda x, *d: x.permute(*d))((x @ wqkv).reshape(B, S, 3, H, Dh), 2, 0, 3, 1, 4)
        q, k, v = qkv[0], qkv[1], qkv[2]
        scores = torch.einsum("bhqd,bhkd->bhqk", q, k) * (Dh ** -0.5)
        p = _softmax(scores, axis=-1)
        o = torch.einsum("bhqk,bhkd->bhqd", p, v)
        return (lambda x, *d: x.permute(*d))(o, 0, 2, 1, 3).reshape(B, S, D) @ wo

    def _B_ref(x, w_gate, w_up, w_down):
        return (_gelu(x @ w_gate) * (x @ w_up)) @ w_down

    x = A_x
    h = x + _A_ref(_layernorm(x, axis=-1), A_wqkv, A_wo)
    h2 = h + _B_ref(_layernorm(h, axis=-1), B_w_gate, B_w_up, B_w_down)
    return h2
```

```cpp
#include <hip/hip_runtime.h>
#include <hip/hip_bf16.h>
#include <stdint.h>
#include <utility>

typedef float f32x16 __attribute__((ext_vector_type(16)));
typedef float f32x4 __attribute__((ext_vector_type(4)));
typedef float f32x2 __attribute__((ext_vector_type(2)));
typedef __bf16 bf16x8 __attribute__((ext_vector_type(8)));
typedef __bf16 bf16x2 __attribute__((ext_vector_type(2)));
typedef short s16x4 __attribute__((ext_vector_type(4)));
typedef unsigned u32x4 __attribute__((ext_vector_type(4)));
typedef unsigned u32x2 __attribute__((ext_vector_type(2)));
#define LAS __attribute__((address_space(3)))
#define GAS __attribute__((address_space(1)))
#define DI __device__ __forceinline__
#define SBAR() __builtin_amdgcn_sched_barrier(0)
#define PIN(x) asm volatile("" : "+v"(x))

constexpr int SEQ = 2048, DM = 576, NH = 32, FF = 4608;
constexpr size_t QIMG = 2304, KVIMG = 4736;
constexpr size_t WS_CNT = 0;
constexpr size_t WS_XMEAN = 16384;
constexpr size_t WS_CMEAN = WS_XMEAN + 8192;
constexpr size_t WS_SSQ = WS_CMEAN + 8192;
constexpr size_t WS_WBAR = WS_SSQ + 32768;
constexpr size_t WS_Q = WS_WBAR + 18432;
constexpr size_t WS_KV = WS_Q + (size_t)NH * 32 * QIMG;
constexpr size_t WS_O = WS_KV + (size_t)NH * 32 * KVIMG;
constexpr size_t WS_HC = WS_O + (size_t)128 * 18 * 1024;
constexpr size_t WS_WO = WS_HC + (size_t)64 * 36 * 1024;
constexpr size_t WS_WGU = WS_WO + (size_t)4 * 18 * 9 * 1024;
constexpr size_t WS_WD = WS_WGU + (size_t)32 * 324 * 1024 + 8192;
constexpr size_t WS_A = WS_WD + (size_t)4 * 144 * 9 * 1024 + 8192;
constexpr size_t WS_XB = WS_A + (size_t)128 * 144 * 1024;
constexpr size_t WS_END = WS_XB + (size_t)64 * 16 * 9216;

DI unsigned rfl(unsigned x) { return __builtin_amdgcn_readfirstlane(x); }
DI unsigned cvtpk(float lo, float hi) { f32x2 v = {lo, hi}; bf16x2 b = __builtin_convertvector(v, bf16x2); return __builtin_bit_cast(unsigned, b); }
DI float bflo(unsigned u) { return __uint_as_float(u << 16); }
DI float bfhi(unsigned u) { return __uint_as_float(u & 0xffff0000u); }
DI f32x16 mfma32(u32x4 a, u32x4 b, f32x16 c) { return __builtin_amdgcn_mfma_f32_32x32x16_bf16(__builtin_bit_cast(bf16x8, a), __builtin_bit_cast(bf16x8, b), c, 0, 0, 0); }
DI f32x16 mfma32k8(u32x2 a, u32x2 b, f32x16 c) { return __builtin_amdgcn_mfma_f32_32x32x8bf16_1k(__builtin_bit_cast(s16x4, a), __builtin_bit_cast(s16x4, b), c, 0, 0, 0); }
DI f32x4 mfma16(u32x4 a, u32x4 b, f32x4 c) { return __builtin_amdgcn_mfma_f32_16x16x32_bf16(__builtin_bit_cast(bf16x8, a), __builtin_bit_cast(bf16x8, b), c, 0, 0, 0); }
DI void glds16(unsigned voff, const void* sbase, unsigned lds) {
    asm volatile("s_mov_b32 m0, %2\n\ts_nop 0\n\tglobal_load_lds_dwordx4 %0, %1" :: "v"(voff), "s"(sbase), "s"(lds) : "memory"); }
template <int OFF> DI void gload16(u32x4& d, unsigned voff, const void* sbase) {
    asm volatile("global_load_dwordx4 %0, %1, %2 offset:%3" : "=v"(d) : "v"(voff), "s"(sbase), "i"(OFF) : "memory"); }
DI void gstore16_wt(unsigned voff, void* sbase, u32x4 v) {
    asm volatile("global_store_dwordx4 %0, %1, %2 sc1\n\ts_nop 1" :: "v"(voff), "v"(v), "s"(sbase) : "memory"); }
template <int N> DI void waitv() { asm volatile("s_waitcnt vmcnt(%0)" :: "i"(N) : "memory"); }
template <int N> DI void waitv_bar() { asm volatile("s_waitcnt vmcnt(%0)\n\ts_barrier" :: "i"(N) : "memory"); }
template <int N> DI void waitv_pin(u32x4& a) { asm volatile("s_waitcnt vmcnt(%1)" : "+v"(a) : "i"(N) : "memory"); }
DI void lbar() { asm volatile("s_waitcnt lgkmcnt(0)\n\ts_barrier" ::: "memory"); }
DI u32x4 lds128(unsigned a) { return *(const LAS u32x4*)(a); }
DI u32x2 lds64(unsigned a) { return *(const LAS u32x2*)(a); }
DI void sts128(unsigned a, u32x4 v) { *(LAS u32x4*)(a) = v; }
DI u32x2 ldstr(unsigned a) { return __builtin_bit_cast(u32x2, __builtin_amdgcn_ds_read_tr16_b64_v4i16((LAS s16x4*)(a))); }
template <int CTRL> DI float dpp_add(float v) { return v + __uint_as_float(__builtin_amdgcn_update_dpp(0u, __float_as_uint(v), CTRL, 0xf, 0xf, true)); }
DI float red8(float v) { v = dpp_add<0xB1>(v); v = dpp_add<0x4E>(v); return dpp_add<0x141>(v); }
DI float red16(float v) { v = red8(v); return dpp_add<0x140>(v); }
template <class F, int... Is> DI void sfor_impl(F&& f, std::integer_sequence<int, Is...>) { (f(std::integral_constant<int, Is>{}), ...); }
template <int N, class F> DI void sfor(F&& f) { sfor_impl(f, std::make_integer_sequence<int, N>{}); }

__host__ __device__ constexpr int vslot(int r) { return r < 3 ? r : r == 3 ? 4 : r < 12 ? r + 5 : r < 16 ? r - 7 : r + 1; }
__host__ __device__ constexpr int k4pos(int t, int ks) { return t >= 3 ? 36 * t + ks : (ks < 24 ? 3 * ks + t : 72 + 12 * t + (ks - 24)); }

#define KT_ARG
#define KT_BEGIN
#define KT_MARK(i)
#define KT_END(slot)

constexpr int K1_XB = 528, K1_X = 0, K1_W = 2 * 72 * K1_XB, K1_BLK = 7168, K1_LDS = K1_W + 12 * K1_BLK;

template <bool VOR> DI void k1_loop(f32x16& c0, f32x16& c1, unsigned xa, unsigned wa, const unsigned (&wv)[7], const char* wq, unsigned lw, int w) {
    sfor<9>([&](auto P) { constexpr int p = P;
        if constexpr (p == 0) { }
        else { if ((w >> 2) == (p & 1)) waitv<0>(); lbar(); }
        if constexpr (p >= 1 && p <= 6) { if ((w >> 2) == ((p + 2) & 1)) {
            const int blk = 4 * (p + 2) + (w & 3); const char* s = wq + (size_t)blk * 16 * 3456; const unsigned l = lw + (blk % 12) * K1_BLK;
            _Pragma("unroll") for (int i = 0; i < 7; ++i) glds16(wv[i], s, l + i * 1024); } }
        sfor<4>([&](auto KK) { constexpr int ks = 4 * p + KK;
            const u32x2 w0 = ldstr(wa + (ks % 12) * K1_BLK), w1 = ldstr(wa + (ks % 12) * K1_BLK + 1792);
            const u32x4 wf = {w0[0], w0[1], w1[0], w1[1]};
            const u32x4 x0 = lds128(xa + ks * 2 * K1_XB), x1 = lds128(xa + (72 + ks * 2) * K1_XB);
            if constexpr (VOR) { c0 = mfma32(x0, wf, c0); c1 = mfma32(x1, wf, c1); }
            else { c0 = mfma32(wf, x0, c0); c1 = mfma32(wf, x1, c1); }
        });
    });
}

__global__ __launch_bounds__(512, 2) void k1_ln_qkv(const char* __restrict__ x, const char* __restrict__ wqkv, char* __restrict__ ws KT_ARG) {
    extern __shared__ __attribute__((aligned(16))) char smem[];
    KT_BEGIN
    const int tid = threadIdx.x, lane = tid & 63, w = rfl(tid >> 6);
    const int xc = blockIdx.x & 7, rk = blockIdx.x >> 3;
    const int mt = (xc >> 2) * 16 + (rk & 15), g = (xc & 3) * 2 + (rk >> 4);
    const unsigned l0 = (unsigned)(uintptr_t)smem;
    if (blockIdx.x == 0 && tid < 64) *(unsigned*)(ws + WS_CNT + tid * 256) = 0u;
    u32x4 xv[9];
    { const unsigned vo = (unsigned)((w * 8 + (lane >> 3)) * 1152 + (lane & 7) * 16); const char* xs = x + (size_t)mt * 64 * 1152;
      sfor<9>([&](auto I) { gload16<I * 128>(xv[I], vo, xs); }); }
    unsigned wv[7];
_Pragma("unroll") for (int i = 0; i < 7; ++i) { const int c = i * 64 + lane, row = c / 28, ch = c - row * 28;
        const int seg = ch < 10 ? 0 : ch < 19 ? 1 : 2, off = ch < 9 ? ch : ch == 9 ? 8 : ch < 19 ? ch - 10 : ch - 19;
        wv[i] = (unsigned)(row * 3456 + seg * 1152 + off * 16); }
    const char* wq = wqkv + g * 144; const unsigned lw = l0 + K1_W;
    { const char* s = wq + (size_t)w * 16 * 3456;
_Pragma("unroll") for (int i = 0; i < 7; ++i) glds16(wv[i], s, lw + w * K1_BLK + i * 1024); }
    if (w < 4) { const char* s = wq + (size_t)(w + 8) * 16 * 3456;
_Pragma("unroll") for (int i = 0; i < 7; ++i) glds16(wv[i], s, lw + (w + 8) * K1_BLK + i * 1024); }
    KT_MARK(0);
    if (w < 4) waitv<14>(); else waitv<7>();
    KT_MARK(1);
    asm volatile("" : "+v"(xv[0]), "+v"(xv[1]), "+v"(xv[2]), "+v"(xv[3]), "+v"(xv[4]), "+v"(xv[5]), "+v"(xv[6]), "+v"(xv[7]), "+v"(xv[8]));
    float xf[72]; float s = 0.f;
_Pragma("unroll") for (int i = 0; i < 9; ++i) _Pragma("unroll") for (int j = 0; j < 4; ++j) { xf[i * 8 + 2 * j] = bflo(xv[i][j]); xf[i * 8 + 2 * j + 1] = bfhi(xv[i][j]); }
    { float s0 = 0, s1 = 0, s2 = 0, s3 = 0;
_Pragma("unroll") for (int i = 0; i < 72; i += 4) { s0 += xf[i]; s1 += xf[i + 1]; s2 += xf[i + 2]; s3 += xf[i + 3]; }
      s = (s0 + s1) + (s2 + s3); }
    const float mean = red8(s) * (1.f / 576.f);
    { float s0 = 0, s1 = 0, s2 = 0, s3 = 0;
_Pragma("unroll") for (int i = 0; i < 72; ++i) xf[i] -= mean;
_Pragma("unroll") for (int i = 0; i < 72; i += 4) { s0 = fmaf(xf[i], xf[i], s0); s1 = fmaf(xf[i + 1], xf[i + 1], s1); s2 = fmaf(xf[i + 2], xf[i + 2], s2); s3 = fmaf(xf[i + 3], xf[i + 3], s3); }
      s = (s0 + s1) + (s2 + s3); }
    const float rstd = __builtin_amdgcn_rsqf(red8(s) * (1.f / 576.f) + 1e-5f);
    { const int row = w * 8 + (lane >> 3); const unsigned xa = l0 + K1_X + ((row >> 5) * 72 + (lane & 7)) * K1_XB + (row & 31) * 16;
_Pragma("unroll") for (int i = 0; i < 9; ++i) { u32x4 o;
_Pragma("unroll") for (int j = 0; j < 4; ++j) o[j] = cvtpk(xf[i * 8 + 2 * j] * rstd, xf[i * 8 + 2 * j + 1] * rstd);
          sts128(xa + i * 8 * K1_XB, o); }
      if (g == 0 && (lane & 7) == 0) *(float*)(ws + WS_XMEAN + (mt * 64 + row) * 4) = mean; }
    KT_MARK(2);
    if (w < 4) waitv<7>();
    lbar();
    KT_MARK(3);
    const int r = lane & 31, hh = lane >> 5;
    const unsigned xa = l0 + K1_X + hh * K1_XB + r * 16;
    unsigned wa; { const int cg = (lane >> 4) & 1, q = (lane & 15) >> 2, p = lane & 3; int cb;
        if (w < 4) cb = 72 * (w >> 1) + (w & 1) * 40 + cg * 160 + 8 * p;
        else if (w == 4) cb = 32 + (p & 1) * 72 + (p >> 1) * 160;
        else if (w < 7) cb = 304 + 64 * (w - 5) + 32 * cg + 8 * p;
        else cb = 304 + 128 + 8 * (p & 1);
        wa = lw + (8 * hh + q) * 448 + cb; }
    f32x16 c0 = {}, c1 = {};
    if (w < 5) k1_loop<false>(c0, c1, xa, wa, wv, wq, lw, w); else k1_loop<true>(c0, c1, xa, wa, wv, wq, lw, w);
    lbar();
    KT_MARK(4);
    constexpr float QS = 0.23570226039551584f * 1.4426950408889634f;
    const unsigned st = lw;
    if (w < 4) { const unsigned hb = st + w * 7040 + lane * 16;
        sts128(hb, u32x4{cvtpk(c0[0] * QS, c0[1] * QS), cvtpk(c0[2] * QS, c0[3] * QS), cvtpk(c0[4] * QS, c0[5] * QS), cvtpk(c0[6] * QS, c0[7] * QS)});
        sts128(hb + 1024, u32x4{cvtpk(c1[0] * QS, c1[1] * QS), cvtpk(c1[2] * QS, c1[3] * QS), cvtpk(c1[4] * QS, c1[5] * QS), cvtpk(c1[6] * QS, c1[7] * QS)});
        sts128(hb + 2304, u32x4{cvtpk(c0[8], c0[9]), cvtpk(c0[10], c0[11]), cvtpk(c0[12], c0[13]), cvtpk(c0[14], c0[15])});
        sts128(hb + 2304 + 1024, u32x4{cvtpk(c1[8], c1[9]), cvtpk(c1[10], c1[11]), cvtpk(c1[12], c1[13]), cvtpk(c1[14], c1[15])});
    } else if (w == 4) {
_Pragma("unroll") for (int e = 0; e < 2; ++e) { const unsigned hb = st + (2 * hh + e) * 7040;
            *(LAS unsigned*)(hb + 2048 + r * 4) = cvtpk(c0[2 * e] * QS, c0[2 * e + 1] * QS);
            *(LAS unsigned*)(hb + 2048 + 128 + r * 4) = cvtpk(c1[2 * e] * QS, c1[2 * e + 1] * QS);
            *(LAS u32x2*)(hb + 2304 + 2048 + r * 8) = u32x2{cvtpk(c0[4 + 2 * e], c0[5 + 2 * e]), cvtpk(c1[4 + 2 * e], c1[5 + 2 * e])}; }
        if (lane < 32) sts128(st + (lane >> 3) * 7040 + 4608 + (lane & 7) * 304 + 48, u32x4{0x3f803f80u, 0x3f803f80u, 0x3f803f80u, 0x3f803f80u});
    } else { const int c = 32 * (w - 5) + r;
        if (c < 72) { const int pr = c / 36, wi = c - pr * 36; const int hd = 2 * pr + (wi >= 18), rho = wi < 18 ? wi : wi < 20 ? wi - 2 : wi - 20;
            const int sl = rho < 3 ? rho : rho == 3 ? 4 : rho < 12 ? rho + 5 : rho < 16 ? rho - 7 : rho + 1;
            const unsigned hb = st + hd * 7040 + 4608 + hh * 304 + sl * 16;
            sts128(hb, u32x4{cvtpk(c0[0], c0[1]), cvtpk(c0[2], c0[3]), cvtpk(c0[4], c0[5]), cvtpk(c0[6], c0[7])});
            sts128(hb + 608, u32x4{cvtpk(c0[8], c0[9]), cvtpk(c0[10], c0[11]), cvtpk(c0[12], c0[13]), cvtpk(c0[14], c0[15])});
            sts128(hb + 1216, u32x4{cvtpk(c1[0], c1[1]), cvtpk(c1[2], c1[3]), cvtpk(c1[4], c1[5]), cvtpk(c1[6], c1[7])});
            sts128(hb + 1824, u32x4{cvtpk(c1[8], c1[9]), cvtpk(c1[10], c1[11]), cvtpk(c1[12], c1[13]), cvtpk(c1[14], c1[15])}); } }
    lbar();
_Pragma("unroll") for (int i = 0; i < 4; ++i) { const int u = tid + 512 * i;
        if (u < 1760) { const int hd = u / 440, o = (u - hd * 440) * 16; const int head = g * 4 + hd;
            const unsigned dst = o < 2304 ? (unsigned)(WS_Q + (head * 32 + mt) * QIMG + o) : (unsigned)(WS_KV + (head * 32 + mt) * KVIMG + (o - 2304));
            gstore16_wt(dst, ws, lds128(st + u * 16)); } }
    KT_END(0)
}

constexpr int K2_LDS = 32 * 4736;

DI void tile8x8(const u32x4 (&in)[8], u32x4 (&out)[8]) {
_Pragma("unroll") for (int c = 0; c < 4; ++c) _Pragma("unroll") for (int k = 0; k < 4; ++k) {
        out[2 * c][k] = __builtin_amdgcn_perm(in[2 * k + 1][c], in[2 * k][c], 0x05040100u);
        out[2 * c + 1][k] = __builtin_amdgcn_perm(in[2 * k + 1][c], in[2 * k][c], 0x07060302u); } }

DI void retile_task(int gw, int lane, const char* wo, const char* wg, const char* wu, const char* wd, char* ws) {
    if (gw < 1296) {
        const int span = gw / 36, ks = gw - span * 36, r = lane & 31, hh = lane >> 5;
        const char* src = (r < 16 ? wg : wu) + (size_t)(16 * ks + 8 * hh) * 9216 + span * 256 + (r & 15) * 16;
        u32x4 in[8], out[8];
_Pragma("unroll") for (int j = 0; j < 8; ++j) in[j] = *(const u32x4*)(src + j * 9216);
        tile8x8(in, out);
_Pragma("unroll") for (int i = 0; i < 8; ++i) { const int T = 8 * span + i, fs = T / 9, t = T - fs * 9;
            gstore16_wt((unsigned)(WS_WGU + (size_t)(fs * 324 + k4pos(t, ks)) * 1024 + lane * 16), ws, out[i]); }
    } else if (gw < 1944) {
        const int r = lane & 15, gq = lane >> 4; const char* src; int lo, hi; unsigned dst;
        if (gw < 1872) { const int id = gw - 1296, j = id & 3, u = id >> 2, T = 2 * u + (gq >> 1);
            src = wd + (size_t)(128 * (T >> 3) + (T & 7) + 32 * (gq & 1)) * 1152 + j * 288; lo = 9216; hi = 73728;
            dst = (unsigned)(WS_WD + (size_t)(j * 144 + u) * 9216);
        } else { const int id = gw - 1872, j = id & 3, u = id >> 2;
            const int row = u < 16 ? 36 * u + (gq >> 1) * 20 + 4 * (gq & 1) : 36 * (8 * (u - 16) + 2 * gq) + 16;
            src = wo + (size_t)row * 1152 + j * 288; lo = 1152; hi = u < 16 ? 9216 : 41472;
            dst = (unsigned)(WS_WO + (size_t)(j * 18 + u) * 9216); }
        u32x4 in[8], out[8]; unsigned short e[8];
_Pragma("unroll") for (int j = 0; j < 8; ++j) { const char* p = src + (j & 3) * lo + (j >> 2) * hi; in[j] = *(const u32x4*)(p + r * 16); e[j] = *(const unsigned short*)(p + 256 + r * 2); }
        tile8x8(in, out);
_Pragma("unroll") for (int i = 0; i < 8; ++i) gstore16_wt(dst + i * 1024 + lane * 16, ws, out[i]);
        gstore16_wt(dst + 8192 + lane * 16, ws, u32x4{e[0] | (unsigned)e[1] << 16, e[2] | (unsigned)e[3] << 16, e[4] | (unsigned)e[5] << 16, e[6] | (unsigned)e[7] << 16});
    } else if (gw < 2016) {
        const int id = gw - 1944, gq = id & 3, u = id >> 2, j = lane >> 3;
        const int row = u < 16 ? 36 * u + (gq >> 1) * 20 + 4 * (gq & 1) + (j & 3) + (j >> 2) * 8 : 36 * (8 * (u - 16) + 2 * gq + (j >> 2)) + 16 + (j & 3);
        const char* src = wo + (size_t)row * 1152 + (lane & 7) * 16; float s = 0.f;
_Pragma("unroll") for (int i = 0; i < 9; ++i) { const u32x4 v = *(const u32x4*)(src + i * 128);
_Pragma("unroll") for (int c = 0; c < 4; ++c) s += bflo(v[c]) + bfhi(v[c]); }
        s = red8(s) * (1.f / 576.f);
        const unsigned hb = cvtpk(s, s) & 0xffffu; const float rest = s - bflo(hb); const unsigned lb = cvtpk(rest, rest) & 0xffffu;
        const unsigned both = hb | lb << 16; u32x4 o;
_Pragma("unroll") for (int c = 0; c < 4; ++c) { const unsigned a = __builtin_amdgcn_readlane(both, 16 * c), b = __builtin_amdgcn_readlane(both, 16 * c + 8);
            o[c] = lane == 0 ? ((a & 0xffffu) | b << 16) : lane == 1 ? ((a >> 16) | (b & 0xffff0000u)) : 0u; }
        if (lane < 16) *(u32x4*)(ws + WS_WBAR + u * 1024 + gq * 256 + lane * 16) = o;
    }
}

DI float xhalf_max(float m) { auto rr = __builtin_amdgcn_permlane32_swap(__float_as_uint(m), __float_as_uint(m), false, false);
    return __builtin_fmaxf(__uint_as_float(rr[0]), __uint_as_float(rr[1])); }
DI float max16(const f32x16& s) { float a = __builtin_fmaxf(__builtin_fmaxf(s[0], s[1]), s[2]), b = __builtin_fmaxf(__builtin_fmaxf(s[3], s[4]), s[5]);
    a = __builtin_fmaxf(__builtin_fmaxf(a, s[6]), s[7]); b = __builtin_fmaxf(__builtin_fmaxf(b, s[8]), s[9]); a = __builtin_fmaxf(__builtin_fmaxf(a, s[10]), s[11]);
    b = __builtin_fmaxf(__builtin_fmaxf(b, s[12]), s[13]); a = __builtin_fmaxf(__builtin_fmaxf(a, s[14]), s[15]); return __builtin_fmaxf(a, b); }

__global__ __launch_bounds__(512, 2) void k2_attn(const char* __restrict__ wo, const char* __restrict__ wg, const char* __restrict__ wu, const char* __restrict__ wd, char* __restrict__ ws KT_ARG) {
    extern __shared__ __attribute__((aligned(16))) char smem[];
    KT_BEGIN
    const int tid = threadIdx.x, lane = tid & 63, w = rfl(tid >> 6), r = lane & 31, hh = lane >> 5;
    const int xc = blockIdx.x & 7, rk = blockIdx.x >> 3, head = 4 * xc + (rk & 3), qb = rk >> 2;
    const unsigned l0 = (unsigned)(uintptr_t)smem;
    const int tt = qb * 8 + w;
    u32x4 qm; unsigned qr;
    { const char* qs = ws + WS_Q + (size_t)(head * 32 + (tt >> 1)) * QIMG + (tt & 1) * 1024;
      gload16<0>(qm, lane * 16, qs);
      const char* q2 = ws + WS_Q + (size_t)(head * 32 + (tt >> 1)) * QIMG + 2048 + (tt & 1) * 128;
      asm volatile("global_load_dword %0, %1, %2" : "=v"(qr) : "v"(r * 4), "s"(q2) : "memory"); }
    { const char* kv = ws + WS_KV + (size_t)head * K2_LDS + w * 1024;
_Pragma("unroll") for (int j = 0; j < 18; ++j) glds16(lane * 16, kv + j * 8192, l0 + w * 1024 + j * 8192);
      if (w < 4) glds16(lane * 16, kv + 18 * 8192, l0 + w * 1024 + 18 * 8192); else glds16(lane * 16, kv + 17 * 8192, l0 + w * 1024 + 17 * 8192); }
    const unsigned ka = l0 + lane * 16, kra = l0 + hh * 4736 + 2048 + r * 8;
    unsigned va; { const int vr = r < 3 ? r : r == 3 ? 4 : r < 12 ? r + 5 : r < 16 ? r - 7 : r < 18 ? r + 1 : 3; va = l0 + 2304 + hh * 304 + vr * 16; }
    waitv<16>(); asm volatile("" : "+v"(qm), "+v"(qr)); asm volatile("s_barrier" ::: "memory");
    KT_MARK(0);
    u32x2 qv[4]; qv[0] = u32x2{hh ? 0u : qr, 0u}; qv[1] = u32x2{0u, hh ? 0u : qr}; qv[2] = u32x2{hh ? qr : 0u, 0u}; qv[3] = u32x2{0u, hh ? qr : 0u};
    f32x16 negm, o = {}, sa, sb; u32x4 p0, p1 = {0u, 0u, 0u, 0u}; u32x2 kr = lds64(kra);
    { f32x16 z = {}; sa = mfma32(lds128(ka), qm, z); sa = mfma32k8(kr, qv[0], sa);
      const float m = xhalf_max(max16(sa));
_Pragma("unroll") for (int i = 0; i < 16; ++i) { negm[i] = -m; sa[i] -= m; } }
    PIN(negm);
    u32x4 kf = lds128(ka + 1024), vf0, vf1 = {0u, 0u, 0u, 0u};
#define EX(v) __builtin_amdgcn_exp2f(v)
#define QUAD(S, i, P, j) do { const float e0 = EX(S[i]), e1 = EX(S[i + 1]), e2 = EX(S[i + 2]), e3 = EX(S[i + 3]); P[j] = cvtpk(e0, e1); P[j + 1] = cvtpk(e2, e3); PIN(P); SBAR(); } while (0)
#define STEP(SC, SN, base, KOFF, VOFF, VAR, LDKR, KROFF) do { \
        vf0 = lds128(va + (base) + (VOFF)); SBAR(); \
        SN = mfma32(kf, qm, negm); SBAR(); kf = lds128(ka + (base) + (KOFF)); QUAD(SC, 0, p0, 0); \
        SN = mfma32k8(kr, qv[VAR], SN); SBAR(); if (LDKR) kr = lds64(kra + (base) + (KROFF)); QUAD(SC, 4, p0, 2); \
        o = mfma32(vf1, p1, o); SBAR(); vf1 = lds128(va + (base) + (VOFF) + 608); QUAD(SC, 8, p1, 0); \
        o = mfma32(vf0, p0, o); SBAR(); QUAD(SC, 12, p1, 2); } while (0)
#define GROUP(base, nb) do { \
        STEP(sa, sb, base, 4736, 0, 1, false, 0); STEP(sb, sa, base, 4736 + 1024, 1216, 2, false, 0); \
        STEP(sa, sb, 0, nb, (base) + 4736, 3, true, nb); STEP(sb, sa, 0, (nb) + 1024, (base) + 4736 + 1216, 0, false, 0); } while (0)
    GROUP(0, 9472);
    waitv_bar<12>();
    for (int gi = 1; gi < 5; ++gi) GROUP(gi * 9472, (gi + 1) * 9472);
    waitv_bar<0>();
    KT_MARK(1);
    retile_task(blockIdx.x * 8 + w, lane, wo, wg, wu, wd, ws);
    KT_MARK(2);
    for (int gi = 5; gi < 16; ++gi) { const int nb = gi == 15 ? 0 : (gi + 1) * 9472; GROUP(gi * 9472, nb); }
    o = mfma32(vf1, p1, o);
    KT_MARK(3);
    { bool bad = !(o[10] < 1e30f);
_Pragma("unroll") for (int i = 0; i < 10; ++i) bad |= !(__builtin_fabsf(o[i]) < 3e38f);
      if (__builtin_expect(__any(bad), 0)) {
        float m = -__builtin_inff(); const f32x16 z = {};
        for (int t = 0; t < 64; ++t) { const unsigned cb = (t >> 1) * 4736; const u32x2 k2 = lds64(l0 + cb + 2048 + r * 8);
            f32x16 s = mfma32(lds128(ka + cb + (t & 1) * 1024), qm, z); s = mfma32k8(k2, (t & 1) ? qv[1] : qv[0], s); m = __builtin_fmaxf(m, max16(s)); }
        m = xhalf_max(m); o = z;
        for (int t = 0; t < 64; ++t) { const unsigned cb = (t >> 1) * 4736; const u32x2 k2 = lds64(l0 + cb + 2048 + r * 8);
            f32x16 s = mfma32(lds128(ka + cb + (t & 1) * 1024), qm, z); s = mfma32k8(k2, (t & 1) ? qv[1] : qv[0], s); u32x4 a, b;
_Pragma("unroll") for (int i = 0; i < 4; ++i) { a[i] = cvtpk(EX(s[2 * i] - m), EX(s[2 * i + 1] - m)); b[i] = cvtpk(EX(s[8 + 2 * i] - m), EX(s[9 + 2 * i] - m)); }
            o = mfma32(lds128(va + cb + (t & 1) * 1216), a, o); o = mfma32(lds128(va + cb + (t & 1) * 1216 + 608), b, o); } } }
    const float il = 1.f / o[10];
    { const unsigned ob = (unsigned)(WS_O + (size_t)((tt * 2 + (r >> 4)) * 18) * 1024 + (r & 15) * 16);
      gstore16_wt(ob + (head >> 1) * 1024 + (2 * (head & 1) + hh) * 256, ws, u32x4{cvtpk(o[0] * il, o[1] * il), cvtpk(o[2] * il, o[3] * il), cvtpk(o[4] * il, o[5] * il), cvtpk(o[6] * il, o[7] * il)});
      if (hh == 0) *(unsigned*)(ws + ob + (16 + (head >> 4)) * 1024 + ((head >> 2) & 3) * 256 + (head & 3) * 4) = cvtpk(o[8] * il, o[9] * il); }
#undef STEP
#undef GROUP
#undef QUAD
    KT_END(1)
}

constexpr int K3_ROW = 164 * 4, K3_LDS = 4 * 32 * K3_ROW;
__global__ __launch_bounds__(512, 2) void k3_oproj(const char* __restrict__ x, char* __restrict__ ws KT_ARG) {
    extern __shared__ __attribute__((aligned(16))) char smem[];
    KT_BEGIN
    const int tid = threadIdx.x, lane = tid & 63, w = rfl(tid >> 6), kg = w & 3, ng = w >> 2;
    const int xc = blockIdx.x & 7, rk = blockIdx.x >> 3, tt = xc * 8 + (rk >> 2), j = rk & 3;
    const unsigned l0 = (unsigned)(uintptr_t)smem;
    const int u0 = kg * 4 + (kg < 2 ? kg : 2), nu = kg < 2 ? 5 : 4;
    u32x4 of[5][2], wf[5][5];
    const char* os = ws + WS_O + (size_t)(tt * 2 * 18 + u0) * 1024;
    const char* wsrc = ws + WS_WO + (size_t)((j * 18 + u0) * 9 + ng * 5) * 1024;
    const char* wb = ws + WS_WBAR + u0 * 1024;
_Pragma("unroll") for (int i = 0; i < 5; ++i) { const int ii = (i == 4 && nu == 4) ? 3 : i;
        of[i][0] = *(const u32x4*)(os + ii * 1024 + lane * 16); of[i][1] = *(const u32x4*)(os + (18 + ii) * 1024 + lane * 16);
_Pragma("unroll") for (int t = 0; t < 5; ++t) { const char* p = (t == 4 && ng == 1) ? wb + ii * 1024 : wsrc + (ii * 9 + t) * 1024; wf[i][t] = *(const u32x4*)(p + lane * 16); } }
    const int etok = tid >> 4, esub = tid & 15;
    const u32x4 xa = *(const u32x4*)(x + (size_t)(tt * 32 + etok) * 1152 + j * 288 + esub * 16);
    const u32x4 xb = *(const u32x4*)(x + (size_t)(tt * 32 + etok) * 1152 + j * 288 + 256 + (esub & 1) * 16);
    const float xm = *(const float*)(ws + WS_XMEAN + (tt * 32 + etok) * 4);
    f32x4 acc[5][2] = {};
_Pragma("unroll") for (int i = 0; i < 5; ++i) { if (i < 4 || nu == 5) {
_Pragma("unroll") for (int t = 0; t < 5; ++t) { acc[t][0] = mfma16(wf[i][t], of[i][0], acc[t][0]); acc[t][1] = mfma16(wf[i][t], of[i][1], acc[t][1]); } } }
    KT_MARK(0);
    { const int q = lane >> 4;
_Pragma("unroll") for (int t = 0; t < 5; ++t) _Pragma("unroll") for (int b = 0; b < 2; ++b) { const int ti = ng * 5 + t;
            const unsigned rowb = l0 + (kg * 32 + b * 16 + (lane & 15)) * K3_ROW;
            if (ti < 8) { _Pragma("unroll") for (int e = 0; e < 4; ++e) *(LAS float*)(rowb + (32 * q + 8 * e + ti) * 4) = acc[t][b][e]; }
            else *(LAS f32x4*)(rowb + (128 + (ti - 8) * 16 + 4 * q) * 4) = acc[t][b]; } }
    __syncthreads();
    KT_MARK(1);
    float cm = xm; float v[8], v2[8];
_Pragma("unroll") for (int e = 0; e < 8; ++e) { v[e] = 0.f; v2[e] = 0.f; }
_Pragma("unroll") for (int k = 0; k < 4; ++k) { const unsigned rowb = l0 + (k * 32 + etok) * K3_ROW;
        const f32x4 a = *(const LAS f32x4*)(rowb + esub * 32), b = *(const LAS f32x4*)(rowb + esub * 32 + 16);
        const f32x4 c = *(const LAS f32x4*)(rowb + 512 + (esub & 1) * 32), d = *(const LAS f32x4*)(rowb + 512 + (esub & 1) * 32 + 16);
        const f32x2 m2 = *(const LAS f32x2*)(rowb + 576); cm += m2[0] + m2[1];
_Pragma("unroll") for (int e = 0; e < 4; ++e) { v[e] += a[e]; v[4 + e] += b[e]; v2[e] += c[e]; v2[4 + e] += d[e]; } }
    u32x4 ha, hb; float sq = 0.f, sq2 = 0.f;
_Pragma("unroll") for (int e = 0; e < 4; ++e) {
        ha[e] = cvtpk(v[2 * e] + bflo(xa[e]) - cm, v[2 * e + 1] + bfhi(xa[e]) - cm); hb[e] = cvtpk(v2[2 * e] + bflo(xb[e]) - cm, v2[2 * e + 1] + bfhi(xb[e]) - cm);
        sq = fmaf(bflo(ha[e]), bflo(ha[e]), sq); sq = fmaf(bfhi(ha[e]), bfhi(ha[e]), sq); sq2 = fmaf(bflo(hb[e]), bflo(hb[e]), sq2); sq2 = fmaf(bfhi(hb[e]), bfhi(hb[e]), sq2); }
    if (esub < 2) sq += sq2;
    sq = red16(sq);
    if (esub == 0) { *(float*)(ws + WS_SSQ + (tt * 32 + etok) * 16 + j * 4) = sq; if (j == 0) *(float*)(ws + WS_CMEAN + (tt * 32 + etok) * 4) = cm; }
    __syncthreads();
    sts128(l0 + esub * 528 + etok * 16, ha); if (esub < 2) sts128(l0 + (16 + esub) * 528 + etok * 16, hb);
    __syncthreads();
    { const unsigned dst = (unsigned)(WS_HC + (size_t)(tt * 36 + j * 9) * 1024);
      gstore16_wt(dst + tid * 16, ws, lds128(l0 + (tid >> 5) * 528 + (tid & 31) * 16));
      if (tid < 64) gstore16_wt(dst + 8192 + tid * 16, ws, lds128(l0 + (16 + (tid >> 5)) * 528 + (tid & 31) * 16)); }
    KT_END(2)
}

constexpr int K4_LDS = 128 * 1024, K4_PF = 4;
__host__ __device__ constexpr int k4_tile(int q) { return q < 72 ? q % 3 : q < 108 ? (q - 72) / 12 : q / 36; }
__host__ __device__ constexpr int k4_ks(int q) { return q < 72 ? q / 3 : q < 108 ? 24 + (q - 72) % 12 : q % 36; }
__host__ __device__ constexpr int k4_xn(int j) { return j < 0 ? 0 : (3 * (j + 1) > 36 ? 36 : 3 * (j + 1)); }
__host__ __device__ constexpr int k4_posW(int j) { return j < 16 ? j + k4_xn(j - 1) : 52 + (j - 16); }
__host__ __device__ constexpr int k4_posX(int ks) { return ks / 3 + 1 + ks; }
__host__ __device__ constexpr int k4_issued(int p) { return p <= 0 ? 52 : (52 + 2 * p > 77 ? 77 : 52 + 2 * p); }
__host__ __device__ constexpr int k4_epi_start(int e) { return e == 0 ? 84 : e == 1 ? 108 : e == 2 ? 132 : e == 3 ? 156 : 36 * (e + 1) + 1; }

__global__ __launch_bounds__(512, 2) void k4_gateup(char* __restrict__ ws KT_ARG) {
    extern __shared__ __attribute__((aligned(16))) char smem[];
    KT_BEGIN
    const int tid = threadIdx.x, lane = tid & 63, w = rfl(tid >> 6), r = lane & 31, hh = lane >> 5;
    const int xc = blockIdx.x & 7, rk = blockIdx.x >> 3, mt = (xc >> 2) * 4 + (rk & 3), fs = (xc & 3) * 8 + (rk >> 2), tt = mt * 8 + w;
    const unsigned l0 = (unsigned)(uintptr_t)smem, lv = lane * 16;
    u32x4 ssq; gload16<0>(ssq, r * 16, ws + WS_SSQ + tt * 512);
    u32x4 xf[36];
    const char* wsrc = ws + WS_WGU + (size_t)fs * 324 * 1024 + w * 1024; const char* xs = ws + WS_HC + (size_t)tt * 36 * 1024;
    sfor<16>([&](auto J) { constexpr int j = J; glds16(lv, wsrc + j * 8192, l0 + w * 1024 + j * 8192);
        if constexpr (j < 12) { gload16<0>(xf[3 * j], lv, xs + j * 3072); gload16<1024>(xf[3 * j + 1], lv, xs + j * 3072); gload16<2048>(xf[3 * j + 2], lv, xs + j * 3072); } });
    waitv_pin<52>(ssq);
    const float var = ((__uint_as_float(ssq[0]) + __uint_as_float(ssq[1])) + (__uint_as_float(ssq[2]) + __uint_as_float(ssq[3]))) * (1.f / 576.f) + 1e-5f;
    const float rs = __builtin_amdgcn_rsqf(var);
    float A1 = -2.f * 0.7978845608028654f * 1.4426950408889634f * rs, A3 = A1 * 0.044715f * rs * rs, IR2 = var;
    PIN(A1); PIN(A3); PIN(IR2);
    unsigned adst = (unsigned)(WS_A + (size_t)((tt * 2 + (r >> 4)) * 144) * 1024 + hh * 256 + (r & 15) * 16);
    unsigned la = l0 + lv, lb = la + 65536; PIN(la); PIN(lb);
    f32x16 acc[3]; u32x4 wf[K4_PF]; u32x4 pk; float ez, ew;
    auto rdw = [&](auto Q) { constexpr int q = Q; constexpr int s = q % 128; wf[q % K4_PF] = lds128((s < 64 ? la : lb) + (s & 63) * 1024); };
    auto epi = [&](auto E, auto PI) { constexpr int e = E, pi = PI, o = pi / 3, st = pi % 3; f32x16& a = acc[e % 3];
        if constexpr (st == 0) { const float g = a[o], t = g * g; ez = g * fmaf(t, A3, A1); ew = g * a[8 + o]; PIN(ez); PIN(ew); }
        if constexpr (st == 1) { ez = fmaf(__builtin_amdgcn_exp2f(ez), IR2, IR2); PIN(ez); }
        if constexpr (st == 2) { a[o] = ew * __builtin_amdgcn_rcpf(ez);
            if constexpr (o & 1) { pk[o >> 1] = cvtpk(a[o - 1], a[o]); PIN(pk); } else PIN(a);
            if constexpr (o == 7) { const int T = fs * 9 + e; gstore16_wt(adst + (T >> 1) * 1024 + (T & 1) * 512, ws, pk); } } };
    sfor<324>([&](auto Q) { constexpr int q = Q, t = k4_tile(q), ks = k4_ks(q);
        if constexpr (q % 16 == 0) { constexpr int p = q / 16, need = 2 * p + 2 > 40 ? 40 : 2 * p + 2;
            waitv_bar<(k4_issued(p - 1) - 1 - k4_posW(need))>();
            if constexpr (p >= 1 && 2 * p + 14 <= 40) glds16(lv, wsrc + (2 * p + 14) * 8192, l0 + w * 1024 + ((2 * p + 14) % 16) * 8192);
            if constexpr (p >= 1 && 2 * p + 15 <= 40) glds16(lv, wsrc + (2 * p + 15) * 8192, l0 + w * 1024 + ((2 * p + 15) % 16) * 8192);
            if constexpr (q == 0) sfor<K4_PF - 1>([&](auto I) { rdw(I); }); }
        if constexpr (q == 0) KT_MARK(0); if constexpr (q == 48) KT_MARK(1); if constexpr (q == 108) KT_MARK(2); if constexpr (q == 216) KT_MARK(3);
        if constexpr (q + K4_PF - 1 < 324) rdw(std::integral_constant<int, q + K4_PF - 1>{});
        if constexpr (t == 0 && q < 84) { constexpr int n = k4_issued(q / 16) - 1 - k4_posX(ks); waitv_pin<(n > 63 ? 63 : n)>(xf[ks]); }
        if constexpr (ks == 0) { const f32x16 z = {}; acc[t % 3] = mfma32(wf[q % K4_PF], xf[ks], z); } else acc[t % 3] = mfma32(wf[q % K4_PF], xf[ks], acc[t % 3]);
        sfor<8>([&](auto E) { constexpr int e = E, s0 = k4_epi_start(e); if constexpr (q >= s0 && q < s0 + 24) epi(E, std::integral_constant<int, q - s0>{}); });
        SBAR();
    });
    KT_MARK(4);
    sfor<24>([&](auto PI) { epi(std::integral_constant<int, 8>{}, PI); });
    KT_END(3)
}

constexpr int K5_LDS = 144 * 1024, K5_AW = 8, K5_ROW = 148 * 4;
__host__ __device__ constexpr int k5_need(int p) { return ((18 * (p + 1) + 7) / 8 - 1) > 40 ? 40 : ((18 * (p + 1) + 7) / 8 - 1); }
__host__ __device__ constexpr int k5_maxr(int p) { return p == 0 ? 15 : ((18 * p + 144) / 8 - 1) > 40 ? 40 : ((18 * p + 144) / 8 - 1); }
__global__ __launch_bounds__(512, 2) void k5_down(char* __restrict__ ws, char* __restrict__ out KT_ARG) {
    extern __shared__ __attribute__((aligned(16))) char smem[];
    KT_BEGIN
    const int tid = threadIdx.x, lane = tid & 63, w = rfl(tid >> 6), tq = w & 3, kg = w >> 2;
    const int xc = blockIdx.x & 7, rk = blockIdx.x >> 3, s4 = xc & 3, m128 = (xc >> 2) * 8 + (rk & 7), j = rk >> 3;
    const unsigned l0 = (unsigned)(uintptr_t)smem, lv = lane * 16;
    const char* wsrc = ws + WS_WD + (size_t)(j * 144 + s4 * 36) * 9216 + w * 1024;
    const char* as = ws + WS_A + (size_t)((m128 * 8 + tq * 2) * 144 + s4 * 36 + kg) * 1024;
    u32x4 af[K5_AW][2];
    sfor<K5_AW>([&](auto P) { constexpr int p = P; gload16<0>(af[p][0], lv, as + p * 2048); gload16<0>(af[p][1], lv, as + p * 2048 + 144 * 1024);
        glds16(lv, wsrc + (2 * p) * 8192, l0 + w * 1024 + (2 * p) * 8192); glds16(lv, wsrc + (2 * p + 1) * 8192, l0 + w * 1024 + (2 * p + 1) * 8192); });
    f32x4 acc[9][2] = {};
    unsigned la = l0 + lv + kg * 9216, lb = la + 54 * 1024, lc = la + 108 * 1024; PIN(la); PIN(lb); PIN(lc);
    sfor<18>([&](auto P) { constexpr int p = P;
        constexpr int nW = k5_need(p);
        constexpr auto issued_before = [](int pp) { int n = 4 * K5_AW; for (int i = 1; i < pp; ++i) { n += k5_maxr(i) - k5_maxr(i - 1); } for (int i = 0; i < pp; ++i) if (i + K5_AW < 18) n += 2; return n; };
        constexpr auto posW = [](int jj) { if (jj < 16) return 4 * (jj / 2) + 2 + (jj & 1); int n = 4 * K5_AW + 2; for (int i = 1; i < 18; ++i) { if (jj <= k5_maxr(i)) return n + (jj - k5_maxr(i - 1) - 1); n += k5_maxr(i) - k5_maxr(i - 1); if (i + K5_AW < 18) n += 2; } return n; };
        constexpr auto posA = [](int pp) { if (pp < K5_AW) return 4 * pp + 1; int n = 4 * K5_AW; for (int i = 0; i < 18; ++i) { if (i >= 1) n += k5_maxr(i) - k5_maxr(i - 1); if (i + K5_AW == pp) return n + 1; if (i + K5_AW < 18) n += 2; } return n; };
        constexpr int last = posW(nW) > posA(p) ? posW(nW) : posA(p);
        asm volatile("s_waitcnt vmcnt(%2)\n\ts_barrier" : "+v"(af[p % K5_AW][0]), "+v"(af[p % K5_AW][1]) : "i"(issued_before(p) - 1 - last) : "memory");
        if constexpr (p >= 1) sfor<k5_maxr(p) - k5_maxr(p - 1)>([&](auto I) { constexpr int jj = k5_maxr(p - 1) + 1 + I; glds16(lv, wsrc + jj * 8192, l0 + w * 1024 + (jj % 18) * 8192); });
        if constexpr (p == 0) KT_MARK(0); if constexpr (p == 9) KT_MARK(1);
        u32x4 wf[9];
        sfor<9>([&](auto T) { constexpr int s = (18 * p + T) % 144; wf[T] = lds128((s < 54 ? la : s < 108 ? lb : lc) + (s % 54) * 1024); });
        sfor<9>([&](auto T) { acc[T][0] = mfma16(wf[T], af[p % K5_AW][0], acc[T][0]); acc[T][1] = mfma16(wf[T], af[p % K5_AW][1], acc[T][1]); });
        SBAR();
        if constexpr (p + K5_AW < 18) { gload16<0>(af[p % K5_AW][0], lv, as + (p + K5_AW) * 2048); gload16<0>(af[p % K5_AW][1], lv, as + (p + K5_AW) * 2048 + 144 * 1024); }
    });
    KT_MARK(2);
    const int etok = tid >> 4, esub = tid & 15, gt = m128 * 128 + s4 * 32 + etok;
    const char* hs = ws + WS_HC + (size_t)((gt >> 5) * 36 + j * 9) * 1024 + etok * 16;
    const u32x4 ha = *(const u32x4*)(hs + esub * 512), hb = *(const u32x4*)(hs + 8192 + (esub & 1) * 512);
    const float cm = *(const float*)(ws + WS_CMEAN + gt * 4);
    lbar();
    { const int q = lane >> 4;
      auto put = [&](bool add) {
_Pragma("unroll") for (int b = 0; b < 2; ++b) { const unsigned rowb = l0 + (tq * 32 + b * 16 + (lane & 15)) * K5_ROW;
_Pragma("unroll") for (int e = 0; e < 4; ++e) _Pragma("unroll") for (int hf = 0; hf < 2; ++hf) { LAS f32x4* p = (LAS f32x4*)(rowb + (32 * q + 8 * e + 4 * hf) * 4);
                f32x4 v = {acc[4 * hf][b][e], acc[4 * hf + 1][b][e], acc[4 * hf + 2][b][e], acc[4 * hf + 3][b][e]}; if (add) v += *p; *p = v; }
            LAS f32x4* p = (LAS f32x4*)(rowb + (128 + 4 * q) * 4); f32x4 v = acc[8][b]; if (add) v += *p; *p = v; } };
      if (kg == 1) put(false);
      lbar();
      if (kg == 0) put(true);
      lbar(); }
    KT_MARK(3);
    const int grp = m128 * 4 + j;
    auto rd8 = [&](int tok, int col, float (&v)[8]) { const f32x4 a = *(const LAS f32x4*)(l0 + tok * K5_ROW + col * 4), b = *(const LAS f32x4*)(l0 + tok * K5_ROW + col * 4 + 16);
_Pragma("unroll") for (int e = 0; e < 4; ++e) { v[e] = a[e]; v[4 + e] = b[e]; } };
_Pragma("unroll") for (int d = 1; d < 4; ++d) { const int dq = (s4 + d) & 3; float v[8];
        const unsigned dst = (unsigned)(WS_XB + (size_t)((grp * 4 + dq) * 4 + s4) * 9216);
        rd8(dq * 32 + etok, esub * 8, v); gstore16_wt(dst + tid * 16, ws, u32x4{cvtpk(v[0], v[1]), cvtpk(v[2], v[3]), cvtpk(v[4], v[5]), cvtpk(v[6], v[7])});
        if (tid < 64) { rd8(dq * 32 + (tid >> 1), 128 + (tid & 1) * 8, v); gstore16_wt(dst + 8192 + tid * 16, ws, u32x4{cvtpk(v[0], v[1]), cvtpk(v[2], v[3]), cvtpk(v[4], v[5]), cvtpk(v[6], v[7])}); } }
    float va[8], vb[8];
    rd8(s4 * 32 + etok, esub * 8, va); rd8(s4 * 32 + etok, 128 + (esub & 1) * 8, vb);
_Pragma("unroll") for (int e = 0; e < 4; ++e) { va[2 * e] += bflo(ha[e]) + cm; va[2 * e + 1] += bfhi(ha[e]) + cm; vb[2 * e] += bflo(hb[e]) + cm; vb[2 * e + 1] += bfhi(hb[e]) + cm; }
    waitv_bar<0>();
    KT_MARK(4);
    GAS unsigned* cnt = (GAS unsigned*)(ws + WS_CNT + grp * 256);
    if (tid == 0) { __hip_atomic_fetch_add(cnt, 1u, __ATOMIC_RELAXED, __HIP_MEMORY_SCOPE_AGENT);
        unsigned spins = 0; while (__hip_atomic_load(cnt, __ATOMIC_RELAXED, __HIP_MEMORY_SCOPE_AGENT) < 4u && ++spins < (1u << 22)) __builtin_amdgcn_s_sleep(1); }
    waitv_bar<0>();
    KT_MARK(5);
    u32x4 pa[3], pb[3];
_Pragma("unroll") for (int d = 1; d < 4; ++d) { const unsigned src = (unsigned)(WS_XB + (size_t)((grp * 4 + s4) * 4 + ((s4 + d) & 3)) * 9216);
        asm volatile("global_load_dwordx4 %0, %1, %2 sc1" : "=v"(pa[d - 1]) : "v"(src + tid * 16), "s"(ws) : "memory");
        asm volatile("global_load_dwordx4 %0, %1, %2 sc1" : "=v"(pb[d - 1]) : "v"(src + 8192 + (etok * 2 + (esub & 1)) * 16), "s"(ws) : "memory"); }
    asm volatile("s_waitcnt vmcnt(0)" : "+v"(pa[0]), "+v"(pa[1]), "+v"(pa[2]), "+v"(pb[0]), "+v"(pb[1]), "+v"(pb[2]) :: "memory");
_Pragma("unroll") for (int d = 0; d < 3; ++d) _Pragma("unroll") for (int e = 0; e < 4; ++e) {
        va[2 * e] += bflo(pa[d][e]); va[2 * e + 1] += bfhi(pa[d][e]); vb[2 * e] += bflo(pb[d][e]); vb[2 * e + 1] += bfhi(pb[d][e]); }
    char* od = out + (size_t)gt * 1152 + j * 288;
    *(u32x4*)(od + esub * 16) = u32x4{cvtpk(va[0], va[1]), cvtpk(va[2], va[3]), cvtpk(va[4], va[5]), cvtpk(va[6], va[7])};
    if (esub < 2) *(u32x4*)(od + 256 + esub * 16) = u32x4{cvtpk(vb[0], vb[1]), cvtpk(vb[2], vb[3]), cvtpk(vb[4], vb[5]), cvtpk(vb[6], vb[7])};
    KT_END(4)
}

#define KT_PASS

extern "C" void kernel_launch(void* const* d_in, const int* in_sizes, int n_in, void* d_out, int out_size, void* d_ws, size_t ws_size, hipStream_t stream) {
    const char* x = (const char*)d_in[0]; const char* wqkv = (const char*)d_in[1]; const char* wo = (const char*)d_in[2];
    const char* wg = (const char*)d_in[4]; const char* wu = (const char*)d_in[5]; const char* wd = (const char*)d_in[6];
    char* ws = (char*)d_ws;
    (void)hipFuncSetAttribute((const void*)k1_ln_qkv, hipFuncAttributeMaxDynamicSharedMemorySize, K1_LDS);
    (void)hipFuncSetAttribute((const void*)k2_attn, hipFuncAttributeMaxDynamicSharedMemorySize, K2_LDS);
    (void)hipFuncSetAttribute((const void*)k3_oproj, hipFuncAttributeMaxDynamicSharedMemorySize, K3_LDS);
    (void)hipFuncSetAttribute((const void*)k4_gateup, hipFuncAttributeMaxDynamicSharedMemorySize, K4_LDS);
    (void)hipFuncSetAttribute((const void*)k5_down, hipFuncAttributeMaxDynamicSharedMemorySize, K5_LDS);
    k1_ln_qkv<<<256, 512, K1_LDS, stream>>>(x, wqkv, ws KT_PASS);
    k2_attn<<<256, 512, K2_LDS, stream>>>(wo, wg, wu, wd, ws KT_PASS);
    k3_oproj<<<256, 512, K3_LDS, stream>>>(x, ws KT_PASS);
    k4_gateup<<<256, 512, K4_LDS, stream>>>(ws KT_PASS);
    k5_down<<<256, 512, K5_LDS, stream>>>(ws, (char*)d_out KT_PASS);
}
```

```cpp
#include <hip/hip_runtime.h>
#include <hip/hip_bf16.h>
#include <stdint.h>
#include <utility>

typedef float f32x16 __attribute__((ext_vector_type(16)));
typedef float f32x4 __attribute__((ext_vector_type(4)));
typedef float f32x2 __attribute__((ext_vector_type(2)));
typedef __bf16 bf16x8 __attribute__((ext_vector_type(8)));
typedef __bf16 bf16x2 __attribute__((ext_vector_type(2)));
typedef short s16x4 __attribute__((ext_vector_type(4)));
typedef unsigned u32x4 __attribute__((ext_vector_type(4)));
typedef unsigned u32x2 __attribute__((ext_vector_type(2)));
typedef unsigned long long u64;
#define LAS __attribute__((address_space(3)))
#define GAS __attribute__((address_space(1)))
#define DI __device__ __forceinline__
#define HD __host__ __device__
#define SBAR() __builtin_amdgcn_sched_barrier(0)
#define PIN(x) asm volatile("" : "+v"(x))
#define UNR _Pragma("unroll")
template <int V> using IC = std::integral_constant<int, V>;

#define ROT1(mt) ((mt) & 7)
constexpr unsigned QIMG = 2304, KVIMG = 4864;
constexpr unsigned WS_EPOCH = 0;
constexpr unsigned WS_FR = 256, WS_FD = WS_FR + 2048;
constexpr unsigned WS_F1 = 8192, WS_F2 = WS_F1 + 32768, WS_F3 = WS_F2 + 32768, WS_F4 = WS_F3 + 32768;
constexpr unsigned WS_XMEAN = WS_F4 + 32768;
constexpr unsigned WS_CM = WS_XMEAN + 8192;
constexpr unsigned WS_SSQ = WS_CM + 8192;
constexpr unsigned WS_WBAR = WS_SSQ + 32768;
constexpr unsigned WS_Q = WS_WBAR + 18432;
constexpr unsigned WS_KV = WS_Q + 32 * 32 * QIMG;
constexpr unsigned WS_O = WS_KV + 32 * 32 * KVIMG;
constexpr unsigned WS_OR = WS_O + 128 * 16 * 1024;
constexpr unsigned WS_HC = WS_OR + 64 * 32 * 128;
constexpr unsigned WS_WO = WS_HC + 64 * 36 * 1024;
constexpr unsigned WS_WGU = WS_WO + 4 * 18 * 9 * 1024;
constexpr unsigned WS_WD = WS_WGU + 32 * 324 * 1024;
constexpr unsigned WS_A = WS_WD + 4 * 144 * 9 * 1024;
constexpr unsigned WS_XB = WS_A + 128 * 144 * 1024;
constexpr unsigned WS_END = WS_XB + 256 * 32768;
constexpr int LDS_BYTES = 163840;

DI unsigned rfl(unsigned x) { return __builtin_amdgcn_readfirstlane(x); }
DI unsigned cvtpk(float lo, float hi) { f32x2 v = {lo, hi}; bf16x2 b = __builtin_convertvector(v, bf16x2); return __builtin_bit_cast(unsigned, b); }
DI float bflo(unsigned u) { return __uint_as_float(u << 16); }
DI float bfhi(unsigned u) { return __uint_as_float(u & 0xffff0000u); }
DI float dot2(unsigned a, unsigned b, float c) { return __builtin_amdgcn_fdot2_f32_bf16(__builtin_bit_cast(bf16x2, a), __builtin_bit_cast(bf16x2, b), c, false); }
DI u32x4 addc8(const u32x4& v, unsigned lo, unsigned hi, float c) { float t0, t1, t2, t3, t4, t5, t6, t7;
    asm("v_dot2_f32_bf16 %0, %8, %12, %14\n\tv_dot2_f32_bf16 %1, %8, %13, %14\n\tv_dot2_f32_bf16 %2, %9, %12, %14\n\tv_dot2_f32_bf16 %3, %9, %13, %14\n\t"
        "v_dot2_f32_bf16 %4, %10, %12, %14\n\tv_dot2_f32_bf16 %5, %10, %13, %14\n\tv_dot2_f32_bf16 %6, %11, %12, %14\n\tv_dot2_f32_bf16 %7, %11, %13, %14\n\ts_nop 2"
        : "=&v"(t0), "=&v"(t1), "=&v"(t2), "=&v"(t3), "=&v"(t4), "=&v"(t5), "=&v"(t6), "=&v"(t7) : "v"(v[0]), "v"(v[1]), "v"(v[2]), "v"(v[3]), "v"(lo), "v"(hi), "v"(c));
    return u32x4{cvtpk(t0, t1), cvtpk(t2, t3), cvtpk(t4, t5), cvtpk(t6, t7)}; }
DI f32x16 mfma32(u32x4 a, u32x4 b, f32x16 c) { return __builtin_amdgcn_mfma_f32_32x32x16_bf16(__builtin_bit_cast(bf16x8, a), __builtin_bit_cast(bf16x8, b), c, 0, 0, 0); }
DI f32x16 mfma32k8(u32x2 a, u32x2 b, f32x16 c) { return __builtin_amdgcn_mfma_f32_32x32x8bf16_1k(__builtin_bit_cast(s16x4, a), __builtin_bit_cast(s16x4, b), c, 0, 0, 0); }
DI f32x4 mfma16(u32x4 a, u32x4 b, f32x4 c) { return __builtin_amdgcn_mfma_f32_16x16x32_bf16(__builtin_bit_cast(bf16x8, a), __builtin_bit_cast(bf16x8, b), c, 0, 0, 0); }
DI void mfma16a(f32x4& c, const u32x4& a, const u32x4& b) { asm volatile("v_mfma_f32_16x16x32_bf16 %0, %1, %2, %0" : "+v"(c) : "v"(a), "v"(b)); }
DI void mfma16z(f32x4& c, const u32x4& a, const u32x4& b) { asm volatile("v_mfma_f32_16x16x32_bf16 %0, %1, %2, 0" : "=&v"(c) : "v"(a), "v"(b)); }
DI void glds16(unsigned voff, const void* sb, unsigned lds) {
    asm volatile("s_mov_b32 m0, %2\n\ts_nop 0\n\tglobal_load_lds_dwordx4 %0, %1" :: "v"(voff), "s"(sb), "s"(lds) : "memory"); }
DI void glds16c(unsigned voff, const void* sb, unsigned lds) {
    asm volatile("s_mov_b32 m0, %2\n\ts_nop 0\n\tglobal_load_lds_dwordx4 %0, %1 sc1" :: "v"(voff), "s"(sb), "s"(lds) : "memory"); }
template <int OFF> DI void gl16(u32x4& d, unsigned voff, const void* sb) { asm volatile("global_load_dwordx4 %0, %1, %2 offset:%3" : "=v"(d) : "v"(voff), "s"(sb), "i"(OFF) : "memory"); }
template <int OFF> DI void gl16c(u32x4& d, unsigned voff, const void* sb) { asm volatile("global_load_dwordx4 %0, %1, %2 offset:%3 sc1" : "=v"(d) : "v"(voff), "s"(sb), "i"(OFF) : "memory"); }
template <int OFF> DI void gl8c(u32x2& d, unsigned voff, const void* sb) { asm volatile("global_load_dwordx2 %0, %1, %2 offset:%3 sc1" : "=v"(d) : "v"(voff), "s"(sb), "i"(OFF) : "memory"); }
template <int OFF> DI void gl4c(unsigned& d, unsigned voff, const void* sb) { asm volatile("global_load_dword %0, %1, %2 offset:%3 sc1" : "=v"(d) : "v"(voff), "s"(sb), "i"(OFF) : "memory"); }
DI void gs16c(unsigned voff, void* sb, u32x4 v) { asm volatile("global_store_dwordx4 %0, %1, %2 sc1\n\ts_nop 1" :: "v"(voff), "v"(v), "s"(sb) : "memory"); }
DI void gs8c(unsigned voff, void* sb, u32x2 v) { asm volatile("global_store_dwordx2 %0, %1, %2 sc1" :: "v"(voff), "v"(v), "s"(sb) : "memory"); }
DI void gs4c(unsigned voff, void* sb, unsigned v) { asm volatile("global_store_dword %0, %1, %2 sc1" :: "v"(voff), "v"(v), "s"(sb) : "memory"); }
template <int N> DI void waitv() { asm volatile("s_waitcnt vmcnt(%0)" :: "i"(N) : "memory"); }
template <int N> DI void waitv_bar() { asm volatile("s_waitcnt vmcnt(%0)\n\ts_barrier" :: "i"(N) : "memory"); }
template <int N> DI void waitv_pin(u32x4& a) { asm volatile("s_waitcnt vmcnt(%1)" : "+v"(a) : "i"(N) : "memory"); }
DI void lbar() { asm volatile("s_waitcnt lgkmcnt(0)\n\ts_barrier" ::: "memory"); }
DI void fullbar() { asm volatile("s_waitcnt vmcnt(0) lgkmcnt(0)\n\ts_barrier" ::: "memory"); }
DI u32x4 lds128(unsigned a) { return *(const LAS u32x4*)(a); }
DI u32x2 lds64(unsigned a) { return *(const LAS u32x2*)(a); }
DI void sts128(unsigned a, u32x4 v) { *(LAS u32x4*)(a) = v; }
DI u32x2 ldstr(unsigned a) { return __builtin_bit_cast(u32x2, __builtin_amdgcn_ds_read_tr16_b64_v4i16((LAS s16x4*)(a))); }
template <int CTRL> DI float dpp_add(float v) { return v + __uint_as_float(__builtin_amdgcn_update_dpp(0u, __float_as_uint(v), CTRL, 0xf, 0xf, true)); }
DI float red8(float v) { v = dpp_add<0xB1>(v); v = dpp_add<0x4E>(v); return dpp_add<0x141>(v); }
DI float red16(float v) { v = red8(v); return dpp_add<0x140>(v); }
template <class F, int... Is> DI void sfor_impl(F&& f, std::integer_sequence<int, Is...>) { (f(IC<Is>{}), ...); }
template <int N, class F> DI void sfor(F&& f) { sfor_impl(f, std::make_integer_sequence<int, N>{}); }
DI int lane_id() { int l; asm volatile("v_mbcnt_lo_u32_b32 %0, -1, 0\n\tv_mbcnt_hi_u32_b32 %0, -1, %0" : "=v"(l)); return l; }

DI void set_flag(char* ws, unsigned off, unsigned tlo, unsigned thi) { gs8c(off, ws, u32x2{tlo, thi}); }
DI void poll_flags(const char* ws, unsigned off, bool need, unsigned tlo, unsigned thi) {
    for (unsigned spins = 0; spins < (1u << 20); ++spins) { u32x2 v; gl8c<0>(v, off, ws); asm volatile("s_waitcnt vmcnt(0)" : "+v"(v) :: "memory");
        if (__all(!need || (v[0] == tlo && v[1] == thi))) break; __builtin_amdgcn_s_sleep(1); } }
DI void poll_flags2(const char* ws, unsigned o1, unsigned o2, unsigned tlo, unsigned thi) {
    for (unsigned spins = 0; spins < (1u << 20); ++spins) { u32x2 v, u; gl8c<0>(v, o1, ws); gl8c<0>(u, o2, ws); asm volatile("s_waitcnt vmcnt(0)" : "+v"(v), "+v"(u) :: "memory");
        if (__all(v[0] == tlo && v[1] == thi && u[0] == tlo && u[1] == thi)) break; __builtin_amdgcn_s_sleep(1); } }
DI void poll_dense(const char* ws, unsigned base, int lane, unsigned tlo, unsigned thi) {
    for (unsigned spins = 0; spins < (1u << 20); ++spins) { u32x4 v, u; gl16c<0>(v, base + lane * 16, ws); gl16c<1024>(u, base + lane * 16, ws); asm volatile("s_waitcnt vmcnt(0)" : "+v"(v), "+v"(u) :: "memory");
        if (__all(v[0] == tlo && v[1] == thi && v[2] == tlo && v[3] == thi && u[0] == tlo && u[1] == thi && u[2] == tlo && u[3] == thi)) break; __builtin_amdgcn_s_sleep(4); } }

HD constexpr int vslot(int r) { return r < 4 ? r : r < 12 ? r + 6 : r < 16 ? r - 6 : r + 2; }
HD constexpr int k4pos(int t, int ks) { return t >= 3 ? 36 * t + ks : (ks < 24 ? 3 * ks + t : 72 + 12 * t + (ks - 24)); }

#define KT_ARG
#define KT_MARK(i)

struct Retile { u32x4 in[8]; unsigned e[8]; };
template <int J> DI void rt_load(Retile& R, const char* p, int lo, int hi) { const char* a = p + ((J & 3) * lo + (J >> 2) * hi);
    asm volatile("global_load_dwordx4 %0, %1, off" : "=v"(R.in[J]) : "v"(a) : "memory"); }
template <int J> DI void rt_load2(Retile& R, const char* p, int lo, int hi) { const char* a = p + ((J & 3) * lo + (J >> 2) * hi);
    asm volatile("global_load_ushort %0, %1, off" : "=v"(R.e[J]) : "v"(a) : "memory"); }
DI void rt_wait(Retile& R) { asm volatile("s_waitcnt vmcnt(0)" : "+v"(R.in[0]), "+v"(R.in[1]), "+v"(R.in[2]), "+v"(R.in[3]), "+v"(R.in[4]), "+v"(R.in[5]), "+v"(R.in[6]), "+v"(R.in[7]) :: "memory"); }
template <int I> DI u32x4 rt_col(const Retile& R) { u32x4 o;
    UNR for (int k = 0; k < 4; ++k) o[k] = __builtin_amdgcn_perm(R.in[2 * k + 1][I >> 1], R.in[2 * k][I >> 1], (I & 1) ? 0x07060302u : 0x05040100u); return o; }
DI u32x4 rt_col9(Retile& R) { asm volatile("" : "+v"(R.e[0]), "+v"(R.e[1]), "+v"(R.e[2]), "+v"(R.e[3]), "+v"(R.e[4]), "+v"(R.e[5]), "+v"(R.e[6]), "+v"(R.e[7]));
    return u32x4{R.e[0] | R.e[1] << 16, R.e[2] | R.e[3] << 16, R.e[4] | R.e[5] << 16, R.e[6] | R.e[7] << 16}; }

constexpr int K1_XB = 528, K1_W = 2 * 72 * K1_XB, K1_BLK = 7168, K1_RS = K1_W + 12 * K1_BLK;

DI int wrap36(int v) { return v >= 36 ? v - 36 : v; }
template <int MODE> DI void p1_loop(f32x16& c0, f32x16& c1, unsigned xa, unsigned xb, unsigned wa, const unsigned (&wv)[7], const char* wq, int w, int rot) {
    u32x4 wf[8], x0[2], x1[2];
    auto rw = [&](auto K) { constexpr int ks = K; const u32x2 w0 = ldstr(wa + (ks % 12) * K1_BLK), w1 = ldstr(wa + (ks % 12) * K1_BLK + 1792); wf[ks % 8] = u32x4{w0[0], w0[1], w1[0], w1[1]}; };
    auto rx = [&](auto K) { constexpr int ks = K; x0[ks & 1] = lds128(xa + ks * 2 * K1_XB); x1[ks & 1] = lds128(xb + ks * 2 * K1_XB); };
    if constexpr (MODE != 2) { sfor<4>(rw); rx(IC<0>{}); }
    sfor<9>([&](auto P) { constexpr int p = P;
        if constexpr (p < 8) { if ((w >> 2) == ((p + 1) & 1)) waitv<0>(); lbar(); }
        const bool own = p <= 5 && (w >> 2) == ((p + 1) & 1);
        const int blk = 4 * (p + 3) + (w & 3); const char* s = wq + wrap36(blk + rot) * (16 * 3456); const unsigned l = K1_W + (blk % 12) * K1_BLK;
        if constexpr (MODE == 2) { if (own) { UNR for (int i = 0; i < 7; ++i) glds16(wv[i], s, l + i * 1024); } }
        else sfor<4>([&](auto KK) { constexpr int ks = 4 * p + KK;
            if constexpr (ks + 1 < 36) rx(IC<ks + 1>{});
            if (own) glds16(wv[2 * KK], s, l + 2 * KK * 1024);
            if constexpr (MODE) c0 = mfma32(x0[ks & 1], wf[ks % 8], c0); else c0 = mfma32(wf[ks % 8], x0[ks & 1], c0);
            SBAR();
            if constexpr (ks + 4 < 36) rw(IC<ks + 4>{});
            if constexpr (KK < 3) { if (own) glds16(wv[2 * KK + 1], s, l + (2 * KK + 1) * 1024); }
            if constexpr (MODE) c1 = mfma32(x1[ks & 1], wf[ks % 8], c1); else c1 = mfma32(wf[ks % 8], x1[ks & 1], c1);
            SBAR(); });
    });
}

DI void phase1(const char* x, const char* wqkv, const char* wo, char* ws, int w, int mt, int ct KT_ARG) {
    const int lane = lane_id(), tid = w * 64 + lane;
    u32x4 xv[9];
    { const unsigned vo = (unsigned)((w * 8 + (lane >> 3)) * 1152 + (lane & 7) * 16); const char* xs = x + (size_t)mt * 64 * 1152;
      sfor<9>([&](auto I) { const int g9 = I + ROT1(mt); gl16<0>(xv[I], vo, xs + (g9 >= 9 ? g9 - 9 : g9) * 128); }); }
    const bool idle = (w == 7 && ct < 5) || (w == 6 && ct > 5); const int id = (ct < 5 ? ct : ct - 1) * 32 + mt; Retile R; u32x4 r9;
    if (idle && id < 144) { const bool fr = id < 72; const int j = id & 3, u = (fr ? id : id - 72) >> 2, rr = lane & 15, gq = fr ? lane >> 4 : j, oc = 4 * (rr & 3) + (rr >> 2), jr = fr ? 0 : lane >> 3;
        const int row = u < 16 ? 36 * u + (gq >> 1) * 20 + 4 * (gq & 1) + (jr & 3) + (jr >> 2) * 8 : 36 * (8 * (u - 16) + 2 * gq + (jr >> 2)) + 16 + (jr & 3);
        const char* p = wo + (row * 1152 + (fr ? j * 288 + oc * 16 : (lane & 7) * 16)); const int lo = fr ? 1152 : 128, hi = fr ? (u < 16 ? 9216 : 41472) : 512;
        sfor<8>([&](auto J) { rt_load<J>(R, p, lo, hi); rt_load2<J>(R, p + (fr ? 256 + rr * 2 - oc * 16 : 0), lo, hi); });
        asm volatile("global_load_dwordx4 %0, %1, off" : "=v"(r9) : "v"(p + (fr ? 0 : 1024)) : "memory"); }
    unsigned wv[7];
    UNR for (int i = 0; i < 7; ++i) { const int c = i * 64 + lane, row = (c * 2341) >> 16, ch = c - row * 28; wv[i] = (unsigned)(row * 3456 + (ch < 27 ? ch : 26) * 16); }
    const int rot = ROT1(mt) * 4; const char* wq = wqkv + ct * 432; const char* wq0 = wq + wrap36(w + rot) * (16 * 3456); const char* wq1 = wq + wrap36(w + 8 + rot) * (16 * 3456);
    asm volatile("s_barrier" ::: "memory");
    auto dm = [&](auto I) { constexpr int i = I; SBAR();
        if constexpr (i < 7) glds16(wv[i], wq0, K1_W + w * K1_BLK + i * 1024);
        else { if (w < 4) glds16(wv[i - 7], wq1, K1_W + (w + 8) * K1_BLK + (i - 7) * 1024); } SBAR(); };
    auto dm1 = [&](auto I) { if (w >= 4) dm(I); };
    if (w < 4) { sfor<7>(dm); waitv<7>(); } else { sfor<2>(dm); waitv<2>(); }
    asm volatile("" : "+v"(xv[0]), "+v"(xv[1]), "+v"(xv[2]), "+v"(xv[3]), "+v"(xv[4]), "+v"(xv[5]), "+v"(xv[6]), "+v"(xv[7]), "+v"(xv[8]));
    KT_MARK(1);
    float mean; const int xrow = w * 8 + (lane >> 3);
    { float s0 = 0, s1 = 0, s2 = 0, s3 = 0; unsigned onev = 0x3f803f80u; PIN(onev);
      sfor<9>([&](auto I) { constexpr int i = I; s0 = dot2(xv[i][0], onev, s0); s1 = dot2(xv[i][1], onev, s1); s2 = dot2(xv[i][2], onev, s2); s3 = dot2(xv[i][3], onev, s3);
          if constexpr (i == 4) { PIN(s0); dm1(IC<2>{}); } });
      mean = red8((s0 + s1) + (s2 + s3)) * (1.f / 576.f); }
    { const unsigned xa = ((xrow >> 5) * 72 + (lane & 7)) * K1_XB + (xrow & 31) * 16;
      unsigned slo = 0x00003f80u, shi = 0x3f800000u; float nm = -mean; PIN(slo); PIN(shi); PIN(nm);
      sfor<9>([&](auto I) { constexpr int i = I; xv[i] = addc8(xv[i], slo, shi, nm); sts128(xa + i * 8 * K1_XB, xv[i]); if constexpr (i % 2 == 0 && i < 8) dm1(IC<3 + i / 2>{}); }); }
    if (w < 4) waitv<0>();
    lbar();
    KT_MARK(2);
    { float q0 = 0, q1 = 0, q2 = 0, q3 = 0;
      sfor<9>([&](auto I) { constexpr int i = I; q0 = dot2(xv[i][0], xv[i][0], q0); q1 = dot2(xv[i][1], xv[i][1], q1); q2 = dot2(xv[i][2], xv[i][2], q2); q3 = dot2(xv[i][3], xv[i][3], q3);
          if constexpr (i < 7) { PIN(q0); dm(IC<7 + i>{}); } });
      const float rstd = __builtin_amdgcn_rsqf(red8((q0 + q1) + (q2 + q3)) * (1.f / 576.f) + 1e-5f);
      if ((lane & 7) == 0) *(LAS float*)(K1_RS + xrow * 4) = rstd; }
    const int r = lane & 31, hh = lane >> 5;
    unsigned xa = hh * K1_XB + r * 16, xb = xa + 72 * K1_XB, wa; PIN(xa); PIN(xb);
    { const int cg = (lane >> 4) & 1, q = (lane & 15) >> 2, p = lane & 3;
      wa = K1_W + (8 * hh + q) * 448 + (w < 6 ? 72 * w + cg * 40 + 8 * p : 32 + 72 * (cg ? 4 + (p & 1) : p)); }
    f32x16 c0 = {}, c1 = {};
    const int gp = 6 * ct + w;
    const bool vor = w < 6 ? gp >= 32 : w == 7;
    if (idle) {
        p1_loop<2>(c0, c1, xa, xb, wa, wv, wq, w, rot);
        if (id < 144) { asm volatile("" : "+v"(R.in[0]), "+v"(R.in[1]), "+v"(R.in[2]), "+v"(R.in[3]), "+v"(R.in[4]), "+v"(R.in[5]), "+v"(R.in[6]), "+v"(R.in[7]), "+v"(r9));
            if (id < 72) { char* d = ws + WS_WO + ((id & 3) * 18 + (id >> 2)) * 9216;
                sfor<8>([&](auto I) { gs16c(lane * 16, d + I * 1024, rt_col<I>(R)); }); gs16c(lane * 16, d + 8192, rt_col9(R));
            } else { float s = 0.f; UNR for (int c = 0; c < 4; ++c) { s += bflo(r9[c]) + bfhi(r9[c]); UNR for (int i = 0; i < 8; ++i) s += bflo(R.in[i][c]) + bfhi(R.in[i][c]); }
                s = red8(s) * (1.f / 576.f);
                const unsigned hb = cvtpk(s, s) & 0xffffu; const float rest = s - bflo(hb); const unsigned both = hb | (cvtpk(rest, rest) & 0xffff0000u); u32x4 o;
                UNR for (int c = 0; c < 4; ++c) { const unsigned a = __builtin_amdgcn_readlane(both, 16 * c), b = __builtin_amdgcn_readlane(both, 16 * c + 8);
                    const unsigned v0 = (a & 0xffffu) | b << 16, v1 = (a >> 16) | (b & 0xffff0000u); o[c] = lane == 0 ? v0 : lane == 1 ? v1 : 0u; }
                if (lane < 16) gs16c(WS_WBAR + (id - 72) * 256 + lane * 16, ws, o); } }
    } else if (vor) p1_loop<1>(c0, c1, xa, xb, wa, wv, wq, w, rot); else p1_loop<0>(c0, c1, xa, xb, wa, wv, wq, w, rot);
    KT_MARK(3);
    constexpr float QS = 0.23570226039551584f * 1.4426950408889634f;
    constexpr unsigned st = K1_W, HB = 2560;
#define PK8(c, o, f) u32x4{cvtpk(c[o] * f, c[o + 1] * f), cvtpk(c[o + 2] * f, c[o + 3] * f), cvtpk(c[o + 4] * f, c[o + 5] * f), cvtpk(c[o + 6] * f, c[o + 7] * f)}
    if (!idle) {
        if (!vor) { const float r0 = *(const LAS float*)(K1_RS + r * 4), r1 = *(const LAS float*)(K1_RS + 128 + r * 4);
            if (w < 6) { const float f = gp < 16 ? QS : 1.f, f0 = f * r0, f1 = f * r1; const unsigned hb = st + 2 * w * HB + lane * 16;
                sts128(hb, PK8(c0, 0, f0)); sts128(hb + 1024, PK8(c1, 0, f1)); sts128(hb + HB, PK8(c0, 8, f0)); sts128(hb + HB + 1024, PK8(c1, 8, f1));
            } else { UNR for (int i = 0; i < 3; ++i) { const int pl = 2 * i + hh, g2 = 6 * ct + pl; const float f = g2 < 16 ? QS : 1.f, f0 = f * r0, f1 = f * r1;
                    UNR for (int e = 0; e < 2; ++e) { const unsigned hb = st + (2 * pl + e) * HB + 2048;
                        const unsigned v0 = cvtpk(c0[4 * i + 2 * e] * f0, c0[4 * i + 2 * e + 1] * f0), v1 = cvtpk(c1[4 * i + 2 * e] * f1, c1[4 * i + 2 * e + 1] * f1);
                        if (g2 < 32) { *(LAS unsigned*)(hb + (g2 < 16 ? r * 4 : r * 8)) = v0; *(LAS unsigned*)(hb + (g2 < 16 ? 128 + r * 4 : r * 8 + 4)) = v1; } } } }
        } else { const int hd = w < 6 ? 2 * w + (r >> 4) : r >> 1, rho = w < 6 ? r & 15 : 16 + (r & 1);
            UNR for (int i = 0; i < 4; ++i) { const f32x4 r0 = *(const LAS f32x4*)(K1_RS + i * 32 + hh * 16), r1 = *(const LAS f32x4*)(K1_RS + 128 + i * 32 + hh * 16);
                UNR for (int e = 0; e < 4; ++e) { c0[4 * i + e] *= r0[e]; c1[4 * i + e] *= r1[e]; } }
            if (w < 6 || (r < 24 && 6 * ct + (r >> 2) >= 32)) { const unsigned hb = st + hd * HB + hh * 320 + vslot(rho) * 16;
                sts128(hb, PK8(c0, 0, 1.f)); sts128(hb + 640, PK8(c0, 8, 1.f)); sts128(hb + 1280, PK8(c1, 0, 1.f)); sts128(hb + 1920, PK8(c1, 8, 1.f)); }
            if (w < 6 && r < 16) { const unsigned cv = hh ? 0u : 0x3f803f80u; sts128(st + 2 * w * HB + r * 320 + 64 + hh * 16, u32x4{cv, cv, cv, cv}); } } }
#undef PK8
    if (ct == 0 && (lane & 7) == 0) gs4c(WS_XMEAN + (mt * 64 + xrow) * 4, ws, __float_as_uint(mean));
    lbar();
    UNR for (int i = 0; i < 4; ++i) { const int u = tid + 512 * i, hd = (u * 6554) >> 20, o = (u - hd * 160) * 16, gh = 12 * ct + hd, sec = gh >> 5, hm = (gh & 31) * 32 + mt;
        if (u < 1920 && (sec == 2 || o < 2304)) gs16c(sec == 0 ? WS_Q + hm * QIMG + o : WS_KV + hm * KVIMG + (sec == 2 ? 2304 : 0) + o, ws, lds128(st + u * 16)); }
}

constexpr int K2_IMG = 32 * KVIMG, K2_GRP = 2 * KVIMG;
struct P3Pre { u32x4 wf[5][5], xa, xb; unsigned xm; };

DI float xhalf_max(float m) { auto rr = __builtin_amdgcn_permlane32_swap(__float_as_uint(m), __float_as_uint(m), false, false);
    return __builtin_fmaxf(__uint_as_float(rr[0]), __uint_as_float(rr[1])); }
DI float max16(const f32x16& s) { float a = __builtin_fmaxf(__builtin_fmaxf(s[0], s[1]), s[2]), b = __builtin_fmaxf(__builtin_fmaxf(s[3], s[4]), s[5]);
    a = __builtin_fmaxf(__builtin_fmaxf(a, s[6]), s[7]); b = __builtin_fmaxf(__builtin_fmaxf(b, s[8]), s[9]); a = __builtin_fmaxf(__builtin_fmaxf(a, s[10]), s[11]);
    b = __builtin_fmaxf(__builtin_fmaxf(b, s[12]), s[13]); a = __builtin_fmaxf(__builtin_fmaxf(a, s[14]), s[15]); return __builtin_fmaxf(a, b); }

DI void phase2(const char* x, const char* wg, const char* wu, const char* wd, char* ws, P3Pre& P, int w, int head, int qb, int t3, int j3, int xb5, unsigned tlo, unsigned thi KT_ARG) {
    const int lane = lane_id(), r = lane & 31, hh = lane >> 5, tt = qb * 8 + w, gw = blockIdx.x * 8 + w;
    const unsigned lv = lane * 16;
    u32x4 qm; unsigned qr;
    { const char* qs = ws + WS_Q + (head * 32 + (tt >> 1)) * QIMG; gl16c<0>(qm, lv, qs + (tt & 1) * 1024); gl4c<2048>(qr, r * 4, qs + (tt & 1) * 128); }
    const char* kv = ws + WS_KV + head * K2_IMG + w * 1024;
    UNR for (int j = 0; j < 4; ++j) glds16c(lv, kv + j * 8192, w * 1024 + j * 8192);
    unsigned ka = lv, kra = hh * KVIMG + 2048 + r * 8, va = 2304 + hh * 320 + (r < 18 ? vslot(r) : (r == 18 || r == 22) ? 4 : 5) * 16;
    if (w == 0 && lane == 0) *(LAS unsigned*)(K2_IMG + 7680) = 0u;
    const char* rp; int rhi, reo; unsigned rd[8]; const bool isgu = gw < 1296, isd = !isgu && gw < 1872;
    { const int span = gw / 36, ks = gw - span * 36, id = isd ? gw - 1296 : 0, rr = lane & 15, gq = lane >> 4, oc = 4 * (rr & 3) + (rr >> 2), T = 2 * (id >> 2) + (gq >> 1);
      rp = isgu ? ((ks & 1) ? wu : wg) + ((32 * (ks >> 1) + 8 * gq) * 9216 + span * 256 + rr * 16) : wd + ((128 * (T >> 3) + (T & 7) + 32 * (gq & 1)) * 1152 + (id & 3) * 288 + oc * 16);
      rhi = isgu ? 36864 : 73728; reo = 256 + rr * 2 - oc * 16;
      const int pa = ks < 24 ? 3 * ks : 48 + ks, pm = ks < 24 ? 1 : 12;
      UNR for (int i = 0; i < 8; ++i) { const int T8 = 8 * span + i, fs = (T8 * 7282) >> 16, t = T8 - fs * 9;
          rd[i] = isgu ? WS_WGU + (fs * 324 + (t >= 3 ? 36 * t + ks : pa + pm * t)) * 1024 : WS_WD + ((id & 3) * 144 + (id >> 2)) * 9216 + i * 1024; } }
    if (w >= 4) __builtin_amdgcn_s_setprio(1);
    asm volatile("s_waitcnt vmcnt(0) lgkmcnt(0)\n\ts_barrier" : "+v"(qm), "+v"(qr) :: "memory");
    KT_MARK(6);
    u32x2 qv[4]; qv[0] = u32x2{hh ? 0u : qr, 0u}; qv[1] = u32x2{0u, hh ? 0u : qr}; qv[2] = u32x2{hh ? qr : 0u, 0u}; qv[3] = u32x2{0u, hh ? qr : 0u};
    f32x16 negm, o = {}, sa, sb; u32x4 p0, p1 = {0u, 0u, 0u, 0u}; u32x2 kr = lds64(kra);
    { f32x16 z = {}; sa = mfma32(lds128(ka), qm, z); sa = mfma32k8(kr, qv[0], sa);
      UNR for (int j = 4; j < 7; ++j) glds16c(lv, kv + j * 8192, w * 1024 + j * 8192);
      const float m = xhalf_max(max16(sa));
      UNR for (int i = 0; i < 16; ++i) { negm[i] = -m; sa[i] -= m; } }
    PIN(negm);
    u32x4 kf = lds128(ka + 1024), vf0, vf1 = {0u, 0u, 0u, 0u};
#define EX(v) __builtin_amdgcn_exp2f(v)
#define QUAD(S, i, P, j) do { const float e0 = EX(S[i]), e1 = EX(S[i + 1]), e2 = EX(S[i + 2]), e3 = EX(S[i + 3]); P[j] = cvtpk(e0, e1); P[j + 1] = cvtpk(e2, e3); PIN(P); SBAR(); } while (0)
#define STEP(SC, SN, KOFF, VOFF, VAR, LDKR, H, H0) do { \
        vf0 = lds128(va + (VOFF)); SBAR(); \
        SN = mfma32(kf, qm, negm); SBAR(); kf = lds128(ka + (KOFF)); QUAD(SC, 0, p0, 0); \
        SN = mfma32k8(kr, qv[VAR], SN); SBAR(); if (LDKR) kr = lds64(kra + K2_GRP); H(IC<H0>{}); QUAD(SC, 4, p0, 2); \
        o = mfma32(vf1, p1, o); SBAR(); vf1 = lds128(va + (VOFF) + 640); H(IC<H0 + 1>{}); QUAD(SC, 8, p1, 0); \
        o = mfma32(vf0, p0, o); SBAR(); H(IC<H0 + 2>{}); QUAD(SC, 12, p1, 2); } while (0)
#define GROUP(H) do { \
        STEP(sa, sb, KVIMG, 0, 1, false, H, 0); STEP(sb, sa, KVIMG + 1024, 1280, 2, false, H, 3); \
        STEP(sa, sb, K2_GRP, KVIMG, 3, true, H, 6); STEP(sb, sa, K2_GRP + 1024, KVIMG + 1280, 0, false, H, 9); \
        ka += K2_GRP; va += K2_GRP; kra += K2_GRP; PIN(ka); PIN(va); PIN(kra); } while (0)
    auto none = [&](auto) {};
    Retile R;
    { auto h = [&](auto I) { glds16c(lv, kv + (7 + I) * 8192, w * 1024 + (7 + I) * 8192); }; GROUP(h); }
    GROUP(none);
    if (w == 4) poll_dense(ws, WS_FD, lane, tlo, thi);
    waitv_bar<0>();
    { auto h = [&](auto I) { if constexpr (I < 8) { rt_load<I>(R, rp, 9216, rhi); if (isd) rt_load2<I>(R, rp + reo, 9216, rhi); } }; GROUP(h); }
    _Pragma("nounroll") for (int gi = 3; gi < 7; ++gi) GROUP(none);
    rt_wait(R);
    { auto h = [&](auto I) { if constexpr (I < 8) { if (isgu || isd) gs16c(lv, ws + rd[I], rt_col<I>(R)); } if constexpr (I == 8) { if (isd) gs16c(lv, ws + rd[7] + 1024, rt_col9(R)); } }; GROUP(h); }
    _Pragma("nounroll") for (int gi = 8; gi < 10; ++gi) GROUP(none);
    __builtin_amdgcn_s_setprio(0);
    { waitv<0>(); unsigned old = 0; if (lane == 0) old = __hip_atomic_fetch_add((LAS unsigned*)(K2_IMG + 7680), 1u, __ATOMIC_RELAXED, __HIP_MEMORY_SCOPE_WORKGROUP);
      if (lane == 0 && old == 7u) set_flag(ws, WS_FR + blockIdx.x * 8, tlo, thi); }
    { const int kg = w & 3, ng = w >> 2, u0 = kg * 4 + (kg < 2 ? kg : 2), tid = w * 64 + lane;
      const char* wsrc = ws + WS_WO + ((j3 * 18 + u0) * 9 + ng * 4) * 1024; const char* w5 = ng ? ws + WS_WBAR + u0 * 1024 : wsrc + 8192; const int s5 = ng ? 1024 : 9216;
      auto ld = [&](auto N) { constexpr int n = N, i = n / 5, t = n % 5; const int ii = (i == 4 && kg >= 2) ? 3 : i;
          if constexpr (n < 25) { if constexpr (t < 4) gl16c<t * 1024>(P.wf[i][t], lv, wsrc + ii * 9216); else gl16c<0>(P.wf[i][4], lv, w5 + ii * s5); }
          if constexpr (n == 25) gl16<0>(P.xa, r * 1152 + (tid >> 5) * 16, x + (size_t)t3 * 32 * 1152 + j3 * 288);
          if constexpr (n == 26) gl16<256>(P.xb, r * 1152 + hh * 16, x + (size_t)t3 * 32 * 1152 + j3 * 288);
          if constexpr (n == 27) gl4c<0>(P.xm, r * 4, ws + WS_XMEAN + t3 * 128); };
      { auto h = [&](auto I) { ld(I); }; GROUP(h); }
      { auto h = [&](auto I) { ld(IC<12 + I>{}); }; GROUP(h); }
      { auto h = [&](auto I) { if constexpr (I < 4) ld(IC<24 + I>{});
                              else if constexpr (I % 2 == 0) gs16c(lv, ws + WS_XB + xb5 * 32768 + (w * 4 + I / 2 - 2) * 1024, u32x4{~0u, ~0u, ~0u, ~0u}); }; GROUP(h); } }
    _Pragma("nounroll") for (int gi = 13; gi < 16; ++gi) GROUP(none);
    o = mfma32(vf1, p1, o);
    KT_MARK(7);
    { bool bad = !(o[10] < 1e30f);
      UNR for (int i = 0; i < 10; ++i) bad |= !(__builtin_fabsf(o[i]) < 3e38f);
      if (__builtin_expect(__any(bad), 0)) {
        float m = -__builtin_inff(); const f32x16 z = {}; va -= K2_IMG;
        for (int t = 0; t < 64; ++t) { const unsigned cb = (t >> 1) * KVIMG; const u32x2 k2 = lds64(cb + 2048 + r * 8);
            f32x16 s = mfma32(lds128(lv + cb + (t & 1) * 1024), qm, z); s = mfma32k8(k2, (t & 1) ? qv[1] : qv[0], s); m = __builtin_fmaxf(m, max16(s)); }
        m = xhalf_max(m); o = z;
        for (int t = 0; t < 64; ++t) { const unsigned cb = (t >> 1) * KVIMG; const u32x2 k2 = lds64(cb + 2048 + r * 8);
            f32x16 s = mfma32(lds128(lv + cb + (t & 1) * 1024), qm, z); s = mfma32k8(k2, (t & 1) ? qv[1] : qv[0], s); u32x4 a, b;
            UNR for (int i = 0; i < 4; ++i) { a[i] = cvtpk(EX(s[2 * i] - m), EX(s[2 * i + 1] - m)); b[i] = cvtpk(EX(s[8 + 2 * i] - m), EX(s[9 + 2 * i] - m)); }
            o = mfma32(lds128(va + cb + (t & 1) * 1280), a, o); o = mfma32(lds128(va + cb + (t & 1) * 1280 + 640), b, o); } } }
    const float il = 1.f / o[10];
    gs16c(WS_O + ((tt * 2 + (r >> 4)) * 16 + (head >> 1)) * 1024 + (2 * (head & 1) + hh) * 256 + (r & 15) * 16, ws,
          u32x4{cvtpk(o[0] * il, o[1] * il), cvtpk(o[2] * il, o[3] * il), cvtpk(o[4] * il, o[5] * il), cvtpk(o[6] * il, o[7] * il)});
    if (hh == 0) gs4c(WS_OR + (tt * 32 + head) * 128 + r * 4, ws, cvtpk(o[8] * il, o[9] * il));
#undef STEP
#undef GROUP
#undef QUAD
}

struct WStream { const char* a; const char* b; unsigned lv, lw; };
template <int J> DI void wround(const WStream& S, int w) {
    const char* p = J < 40 ? S.a + J * 8192 : J > 40 ? S.b + (J - 40) * 8192 : (w < 4 ? S.a + J * 8192 : S.b);
    glds16c(S.lv, p, S.lw + (J % 18) * 8192); }

constexpr int K3_ROW = 164 * 4, K3_L = LDS_BYTES - 4 * 32 * K3_ROW;
DI void phase3(char* ws, P3Pre& P, int w, int tt, int j KT_ARG) {
    const int lane = lane_id(), kg = w & 3, ng = w >> 2;
    const unsigned lv = lane * 16;
    const int u0 = kg * 4 + (kg < 2 ? kg : 2);
    u32x4 of[5][2]; unsigned t[16]; f32x4 acc[5][2] = {};
    { const char* os = ws + WS_O + (tt * 32 + u0) * 1024; const int lim = kg < 2 ? 4 : kg == 2 ? 3 : 1;
      if (kg == 3) { const char* rs = ws + WS_OR + tt * 4096; const unsigned ro = (lane >> 4) * 512 + (lane & 15) * 4;
          sfor<16>([&](auto I) { constexpr int i = I; gl4c<(i >> 3) * 2048 + ((i >> 2) & 1) * 64 + (i & 3) * 128>(t[i], ro, rs); }); }
      sfor<5>([&](auto I) { constexpr int i = I; const int ii = i < lim ? i : lim; gl16c<0>(of[i][0], lv, os + ii * 1024); gl16c<0>(of[i][1], lv, os + ii * 1024 + 16384); }); }
    sfor<5>([&](auto I) { constexpr int i = I;
        asm volatile("s_waitcnt vmcnt(%2)" : "+v"(of[i][0]), "+v"(of[i][1]) : "i"(8 - 2 * i) : "memory");
        if constexpr (i == 2 || i == 3) { if (kg == 3) { constexpr int b = (i - 2) * 8;
            asm volatile("" : "+v"(t[b]), "+v"(t[b + 1]), "+v"(t[b + 2]), "+v"(t[b + 3]), "+v"(t[b + 4]), "+v"(t[b + 5]), "+v"(t[b + 6]), "+v"(t[b + 7]));
            of[i][0] = u32x4{t[b], t[b + 1], t[b + 2], t[b + 3]}; of[i][1] = u32x4{t[b + 4], t[b + 5], t[b + 6], t[b + 7]}; } }
        if (i < 4 || kg < 2) { UNR for (int c = 0; c < 5; ++c) { acc[c][0] = mfma16(P.wf[i][c], of[i][0], acc[c][0]); acc[c][1] = mfma16(P.wf[i][c], of[i][1], acc[c][1]); } }
        SBAR(); });
    KT_MARK(11);
    { const int q = lane >> 4;
      UNR for (int b = 0; b < 2; ++b) { const unsigned rowb = K3_L + (kg * 32 + b * 16 + (lane & 15)) * K3_ROW;
          UNR for (int e = 0; e < 4; ++e) *(LAS f32x4*)(rowb + (32 * e + 8 * q + 4 * ng) * 4) = f32x4{acc[0][b][e], acc[1][b][e], acc[2][b][e], acc[3][b][e]};
          *(LAS f32x4*)(rowb + (128 + ng * 16 + 4 * q) * 4) = acc[4][b]; } }
    lbar();
    const unsigned rb = K3_L + (lane & 31) * K3_ROW; float cm = __uint_as_float(P.xm);
    UNR for (int k = 0; k < 4; ++k) { const f32x2 m2 = *(const LAS f32x2*)(rb + k * 32 * K3_ROW + 576); cm += m2[0] + m2[1]; }
    float sq = 0.f;
    auto unit = [&](unsigned col, const u32x4& xr, unsigned dst) { f32x4 a = {}, b = {};
        UNR for (int k = 0; k < 4; ++k) { a += *(const LAS f32x4*)(rb + k * 32 * K3_ROW + col); b += *(const LAS f32x4*)(rb + k * 32 * K3_ROW + col + 16); }
        const u32x4 hv = {cvtpk(a[0] + bflo(xr[0]) - cm, a[1] + bfhi(xr[0]) - cm), cvtpk(a[2] + bflo(xr[1]) - cm, a[3] + bfhi(xr[1]) - cm),
                          cvtpk(b[0] + bflo(xr[2]) - cm, b[1] + bfhi(xr[2]) - cm), cvtpk(b[2] + bflo(xr[3]) - cm, b[3] + bfhi(xr[3]) - cm)};
        gs16c(dst, ws, hv);
        UNR for (int e = 0; e < 4; ++e) { sq = fmaf(bflo(hv[e]), bflo(hv[e]), sq); sq = fmaf(bfhi(hv[e]), bfhi(hv[e]), sq); } };
    const unsigned dst = WS_HC + (tt * 2 + ((lane >> 4) & 1)) * 18432 + (18 * j + (lane >> 5)) * 256 + (lane & 15) * 16;
    unit((2 * w + (lane >> 5)) * 32, P.xa, dst + w * 512);
    if (w == 7) unit(512 + (lane >> 5) * 32, P.xb, dst + 4096);
    *(LAS float*)(73728 + (w * 64 + lane) * 4) = sq;
    if (w == 6 && j == 0 && lane < 32) gs4c(WS_CM + tt * 128 + lane * 4, ws, __float_as_uint(cm));
    lbar();
    if (w == 5 && lane < 32) { float t2 = 0.f; UNR for (int i = 0; i < 16; ++i) t2 += *(const LAS float*)(73728 + i * 128 + lane * 4);
        gs4c(WS_SSQ + (tt * 4 + j) * 128 + lane * 4, ws, __float_as_uint(t2)); }
}

#define K4PER 24
#define K4X0 4
constexpr int K4_PF = 5, K4_X0 = K4X0, K4_PER = K4PER;
HD constexpr int k4_tile(int q) { return q < 72 ? q % 3 : q < 108 ? (q - 72) / 12 : q / 36; }
HD constexpr int k4_ks(int q) { return q < 72 ? q / 3 : q < 108 ? 24 + (q - 72) % 12 : q % 36; }
HD constexpr int k4_xn(int q) { return q < 0 ? K4_X0 : (K4_X0 + (q + 1) / 2 > 36 ? 36 : K4_X0 + (q + 1) / 2); }
HD constexpr int k4_xslot(int ks) { return ks < K4_X0 ? -1 : 2 * (ks - K4_X0) + 1; }
HD constexpr int k4_wn(int q) { int n = 18; for (int s = 60; s <= q; ++s) if (s % 4 == 2 && 8 * n + 8 <= (s / K4_PER) * K4_PER + 144) ++n; return n; }
HD constexpr int k4_wslot(int j) { if (j < 18) return -2; int q = 0; while (k4_wn(q) <= j) ++q; return q; }
HD constexpr int k4_epi_start(int e) { return e == 0 ? 86 : 108 + 31 * (e - 1); }
HD constexpr int k4_epi_len(int e) { return e == 0 ? 22 : e == 7 ? 30 : 31; }
HD constexpr int k4_epi_first(int e, int gap) { const int g = gap - 2 * k4_epi_start(e), n = 2 * k4_epi_len(e); return g <= 0 ? 0 : g >= n ? 70 : (g * 70 + n - 1) / n; }
constexpr int K4_WEND = k4_wn(323);

DI void phase4(char* ws, const WStream& S, int w, int tt, int fs KT_ARG) {
    const int lane = lane_id();
    const unsigned lv = lane * 16;
    unsigned sq[8]; u32x4 xf[36];
    const char* xs = ws + WS_HC + tt * 36 * 1024;
    auto ldx = [&](auto K) { constexpr int k = K; gl16c<((k >> 1) % 3) * 1024>(xf[k], lv, xs + ((k & 1) * 18 + (k >> 1) / 3 * 3) * 1024); };
    sfor<K4_X0>(ldx);
    { const char* sp = ws + WS_SSQ + tt * 512; sfor<8>([&](auto I) { gl4c<I * 64>(sq[I], (lane & 15) * 4, sp); }); }
    float A1[2], A3[2], IR2[2];
    unsigned adst = WS_A + ((tt * 2 + (lane >> 5)) * 144) * 1024 + (lane & 31) * 16;
    unsigned la = lv, lb = lv + 49152, lc = lv + 98304; PIN(la); PIN(lb); PIN(lc);
    f32x4 acc[3][4]; u32x4 wf[K4_PF]; unsigned pk[4]; float ez[2], ew[2];
    auto rdw = [&](auto Q) { constexpr int q = Q; constexpr int s = q % 144; wf[q % K4_PF] = lds128((s < 48 ? la : s < 96 ? lb : lc) + (s % 48) * 1024); };
    auto epi = [&](auto E, auto I) { constexpr int e = E, i = I, pr = i / 17, st = i % 17, b = pr >> 1, h = st & 1, c = (pr & 1) * 2 + h; f32x4 (&a)[4] = acc[e % 3];
        if constexpr (i < 68) {
            if constexpr (st < 2) ez[h] = a[b][c] * a[b][c];
            else if constexpr (st < 4) ez[h] = fmaf(ez[h], A3[b], A1[b]);
            else if constexpr (st < 6) ez[h] *= a[b][c];
            else if constexpr (st < 8) ez[h] = __builtin_amdgcn_exp2f(ez[h]);
            else if constexpr (st < 10) ew[h] = a[b][c] * a[2 + b][c];
            else if constexpr (st < 12) ez[h] = fmaf(ez[h], IR2[b], IR2[b]);
            else if constexpr (st < 14) ez[h] = __builtin_amdgcn_rcpf(ez[h]);
            else if constexpr (st < 16) ew[h] *= ez[h];
            else pk[pr] = cvtpk(ew[0], ew[1]);
            if constexpr (st == 16) PIN(pk[pr]); else if constexpr (st == 8 || st == 9 || st >= 14) PIN(ew[h]); else PIN(ez[h]);
        } else if constexpr (i == 68) { const auto r0 = __builtin_amdgcn_permlane32_swap(pk[0], pk[2], false, false); pk[0] = r0[0]; pk[2] = r0[1]; PIN(pk[0]); PIN(pk[2]); }
        else { const int T = fs * 9 + e; const auto r1 = __builtin_amdgcn_permlane32_swap(pk[1], pk[3], false, false);
            gs16c(adst, ws + (T >> 1) * 1024 + (T & 1) * 512, u32x4{pk[0], r1[0], pk[2], r1[1]}); } };
    auto gap = [&](auto G) { sfor<8>([&](auto E) { constexpr int f = k4_epi_first(E, G), l = k4_epi_first(E, G + 1); sfor<l - f>([&](auto I) { epi(E, IC<f + I>{}); }); }); SBAR(); };
    sfor<K4_PF - 1>([&](auto I) { rdw(I); });
    sfor<324>([&](auto Q) { constexpr int q = Q, t = k4_tile(q), ks = k4_ks(q), x0 = ks & ~1, m = ks & 1;
        if constexpr (q % K4_PER == 0 && q > 0) { constexpr int need = (q + K4_PER + K4_PF + 6) / 8 - 1 > 40 ? 40 : (q + K4_PER + K4_PF + 6) / 8 - 1;
            constexpr int n = (k4_wn(q - 1) - need - 1) + (k4_xn(q - 1) - (need < 18 ? 0 : k4_xn(k4_wslot(need) - 1)));
            static_assert(need < k4_wn(q - 1), "needed round not issued");
            waitv_bar<(n > 63 ? 63 : n)>(); }
        if constexpr (k4_wn(q) > k4_wn(q - 1)) wround<k4_wn(q) - 1>(S, w);
        if constexpr (k4_xn(q) > k4_xn(q - 1)) ldx(IC<k4_xn(q) - 1>{});
        if constexpr (q == 108) KT_MARK(14); if constexpr (q == 216) KT_MARK(15);
        if constexpr (t == 0 && q < 84 && m == 0) { constexpr int n = (k4_xn(q) - ks - 2) + (k4_wn(q) - k4_wn(k4_xslot(ks + 1))) + (ks + 1 < K4_X0 ? 8 : 0);
            static_assert(k4_xn(q) >= ks + 2, "X not issued");
            asm volatile("s_waitcnt vmcnt(%2)" : "+v"(xf[ks]), "+v"(xf[ks + 1]) : "i"(n) : "memory"); }
        if constexpr (ks < 2) mfma16z(acc[t % 3][2 * m], wf[q % K4_PF], xf[x0]); else mfma16a(acc[t % 3][2 * m], wf[q % K4_PF], xf[x0]);
        SBAR();
        if constexpr (q == 40) { asm volatile("" : "+v"(sq[0]), "+v"(sq[1]), "+v"(sq[2]), "+v"(sq[3]), "+v"(sq[4]), "+v"(sq[5]), "+v"(sq[6]), "+v"(sq[7]));
            UNR for (int b = 0; b < 2; ++b) {
                const float var = ((__uint_as_float(sq[b]) + __uint_as_float(sq[2 + b])) + (__uint_as_float(sq[4 + b]) + __uint_as_float(sq[6 + b]))) * (1.f / 576.f) + 1e-5f;
                const float rs = __builtin_amdgcn_rsqf(var);
                A1[b] = -2.f * 0.7978845608028654f * 1.4426950408889634f * rs; A3[b] = A1[b] * 0.044715f * rs * rs; IR2[b] = var; PIN(A1[b]); PIN(A3[b]); PIN(IR2[b]); } }
        gap(IC<2 * q>{});
        if constexpr (q + K4_PF - 1 < 324) rdw(IC<q + K4_PF - 1>{});
        SBAR();
        if constexpr (ks < 2) mfma16z(acc[t % 3][2 * m + 1], wf[q % K4_PF], xf[x0 + 1]); else mfma16a(acc[t % 3][2 * m + 1], wf[q % K4_PF], xf[x0 + 1]);
        SBAR();
        gap(IC<2 * q + 1>{});
    });
    KT_MARK(16);
    asm volatile("s_nop 7" : "+v"(acc[2][0]), "+v"(acc[2][1]), "+v"(acc[2][2]), "+v"(acc[2][3]));
    sfor<70>([&](auto I) { epi(IC<8>{}, I); });
}

#define K5A0 4
#define K5AW 4
constexpr int K5_PF = 5, K5_AW = K5AW, K5_A0 = K5A0, K5_W0 = 58;
HD constexpr int k5_wn(int q) { int n = K5_W0; for (int s = 18; s <= q; ++s) if (s % 3 == 1 && n < 81 && 8 * n + 8 <= 324 + (s / 18) * 36 + 144) ++n; return n; }
HD constexpr int k5_wslot(int j) { if (j < K5_W0) return -2; int q = 0; while (k5_wn(q) <= j) ++q; return q; }
HD constexpr int k5_an(int q) { int n = K5_A0; for (int s = 0; s <= q; ++s) if (s % 2 == 1 && n < 36 && n / 2 < s / 9 + K5_AW) ++n; return n; }
HD constexpr int k5_aslot(int i) { if (i < K5_A0) return -1; int q = 0; while (k5_an(q) <= i) ++q; return q; }

DI void phase5(char* ws, char* out, const WStream& S, int w, int m128, int j, int s4 KT_ARG) {
    const int lane = lane_id(), tq = w & 3, kg = w >> 2;
    const unsigned lv = lane * 16;
    const char* as = ws + WS_A + ((m128 * 8 + tq * 2) * 144 + s4 * 36 + kg) * 1024;
    u32x4 af[K5_AW][2], wf[K5_PF];
    auto lda = [&](auto I) { constexpr int i = I, p = i / 2; gl16c<0>(af[p % K5_AW][i & 1], lv, as + p * 2048 + (i & 1) * 147456); };
    sfor<K5_A0>(lda);
    f32x4 acc[9][2];
    unsigned la = lv + kg * 9216, lb = la + 54 * 1024, lc = la + 108 * 1024; PIN(la); PIN(lb); PIN(lc);
    auto rdw = [&](auto Q) { constexpr int q = Q, s = (36 + 18 * (q / 9) + q % 9) % 144; wf[q % K5_PF] = lds128((s < 54 ? la : s < 108 ? lb : lc) + (s % 54) * 1024); };
    sfor<K5_PF - 1>([&](auto I) { rdw(I); });
    sfor<162>([&](auto Q) { constexpr int q = Q, p = q / 9, T = q % 9;
        if constexpr (q % 18 == 0 && q > 0) { constexpr int need = (324 + 2 * q + 36 + 18 + 7) / 8 - 1 > 80 ? 80 : (324 + 2 * q + 36 + 18 + 7) / 8 - 1;
            constexpr int n = (k5_wn(q - 1) - need - 1) + (k5_an(q - 1) - (need < K5_W0 ? 0 : k5_an(k5_wslot(need) - 1)));
            static_assert(need < k5_wn(q - 1), "needed round not issued");
            waitv_bar<(n > 63 ? 63 : n)>(); }
        if constexpr (k5_wn(q) > k5_wn(q - 1)) wround<k5_wn(q) - 1>(S, w);
        if constexpr (k5_an(q) > k5_an(q - 1)) lda(IC<k5_an(q) - 1>{});
        if constexpr (q == 81) KT_MARK(19);
        if constexpr (T == 0) { constexpr int i = 2 * p + 1, n = (k5_an(q) - i - 1) + (k5_wn(q) - k5_wn(k5_aslot(i))); static_assert(k5_an(q) > i, "a not issued");
            asm volatile("s_waitcnt vmcnt(%2)" : "+v"(af[p % K5_AW][0]), "+v"(af[p % K5_AW][1]) : "i"(n) : "memory"); }
        if constexpr (p == 0) mfma16z(acc[T][0], wf[q % K5_PF], af[0][0]); else mfma16a(acc[T][0], wf[q % K5_PF], af[p % K5_AW][0]);
        SBAR();
        if constexpr (q + K5_PF - 1 < 162) rdw(IC<q + K5_PF - 1>{});
        SBAR();
        if constexpr (p == 0) mfma16z(acc[T][1], wf[q % K5_PF], af[0][1]); else mfma16a(acc[T][1], wf[q % K5_PF], af[p % K5_AW][1]);
        SBAR();
    });
    asm volatile("s_nop 7" : "+v"(acc[7][0]), "+v"(acc[7][1]), "+v"(acc[8][0]), "+v"(acc[8][1]));
    KT_MARK(20);
    const int q = lane >> 4, gt = m128 * 128 + tq * 32 + kg * 16 + (lane & 15), grp = m128 * 4 + j;
    u32x4 hr[4]; u32x2 h9; unsigned cmu;
    if (tq == s4) { const char* hs = ws + WS_HC + (m128 * 8 + tq * 2 + kg) * 18432 + j * 4608;
        gl16c<0>(hr[0], lv, hs); gl16c<1024>(hr[1], lv, hs); gl16c<2048>(hr[2], lv, hs); gl16c<3072>(hr[3], lv, hs);
        gl8c<0>(h9, (q >> 1) * 256 + (lane & 15) * 16 + (q & 1) * 8, hs + 4096); gl4c<0>(cmu, gt * 4, ws + WS_CM); }
    UNR for (int t = 0; t < 9; ++t) *(LAS f32x4*)(73728 + w * 9216 + t * 1024 + lv) = kg ? acc[t][0] : acc[t][1];
    lbar();
    f32x4 v[9];
    UNR for (int t = 0; t < 9; ++t) v[t] = (kg ? acc[t][1] : acc[t][0]) + *(const LAS f32x4*)(73728 + (w ^ 4) * 9216 + t * 1024 + lv);
    KT_MARK(21);
    if (tq != s4) { char* dst = ws + WS_XB + (grp * 4 + tq) * 32768 + (((s4 - tq - 1) & 3) * 2 + kg) * 4608;
        UNR for (int e = 0; e < 4; ++e) gs16c(lv + e * 1024, dst, u32x4{cvtpk(v[0][e], v[1][e]), cvtpk(v[2][e], v[3][e]), cvtpk(v[4][e], v[5][e]), cvtpk(v[6][e], v[7][e])});
        gs8c(4096 + lane * 8, dst, u32x2{cvtpk(v[8][0], v[8][1]), cvtpk(v[8][2], v[8][3])});
        if (lane == 0) gs4c(WS_XB + (grp * 4 + tq) * 32768 + 27648 + (((s4 - tq - 1) & 3) * 2 + kg) * 128, ws, 0u);
    } else {
        asm volatile("s_waitcnt vmcnt(0)" : "+v"(hr[0]), "+v"(hr[1]), "+v"(hr[2]), "+v"(hr[3]), "+v"(h9), "+v"(cmu) :: "memory");
        const float cm = __uint_as_float(cmu);
        UNR for (int e = 0; e < 4; ++e) { UNR for (int i = 0; i < 4; ++i) { v[2 * i][e] += bflo(hr[e][i]) + cm; v[2 * i + 1][e] += bfhi(hr[e][i]) + cm; } }
        v[8][0] += bflo(h9[0]) + cm; v[8][1] += bfhi(h9[0]) + cm; v[8][2] += bflo(h9[1]) + cm; v[8][3] += bfhi(h9[1]) + cm;
        UNR for (int t = 0; t < 9; ++t) PIN(v[t]);
        for (unsigned spins = 0; spins < (1u << 20); ++spins) { unsigned hv; gl4c<0>(hv, WS_XB + (grp * 4 + s4) * 32768 + 27648 + ((lane < 3 ? lane : 0) * 2 + kg) * 128, ws);
            asm volatile("s_waitcnt vmcnt(0)" : "+v"(hv) :: "memory"); if (__all(hv == 0u)) break; }
        u32x4 pa[3][4]; u32x2 pb[3]; const char* src = ws + WS_XB + (grp * 4 + s4) * 32768 + kg * 4608;
        for (unsigned spins = 0; spins < (1u << 16); ++spins) {
            sfor<3>([&](auto D) { constexpr int d = D;
                gl16c<0>(pa[d][0], lv, src + d * 9216); gl16c<1024>(pa[d][1], lv, src + d * 9216); gl16c<2048>(pa[d][2], lv, src + d * 9216); gl16c<3072>(pa[d][3], lv, src + d * 9216); gl8c<0>(pb[d], lane * 8, src + d * 9216 + 4096); });
            asm volatile("s_waitcnt vmcnt(0)" : "+v"(pa[0][0]), "+v"(pa[0][1]), "+v"(pa[0][2]), "+v"(pa[0][3]), "+v"(pa[1][0]), "+v"(pa[1][1]), "+v"(pa[1][2]), "+v"(pa[1][3]),
                         "+v"(pa[2][0]), "+v"(pa[2][1]), "+v"(pa[2][2]), "+v"(pa[2][3]), "+v"(pb[0]), "+v"(pb[1]), "+v"(pb[2]) :: "memory");
            unsigned mx = 0;
            UNR for (int d = 0; d < 3; ++d) { UNR for (int e = 0; e < 4; ++e) UNR for (int i = 0; i < 4; ++i) mx = mx > pa[d][e][i] ? mx : pa[d][e][i];
                mx = mx > pb[d][0] ? mx : pb[d][0]; mx = mx > pb[d][1] ? mx : pb[d][1]; }
            if (__all(mx != ~0u)) break; }
        UNR for (int d = 0; d < 3; ++d) { UNR for (int e = 0; e < 4; ++e) UNR for (int i = 0; i < 4; ++i) { v[2 * i][e] += bflo(pa[d][e][i]); v[2 * i + 1][e] += bfhi(pa[d][e][i]); }
            v[8][0] += bflo(pb[d][0]); v[8][1] += bfhi(pb[d][0]); v[8][2] += bflo(pb[d][1]); v[8][3] += bfhi(pb[d][1]); }
        char* od = out + (size_t)gt * 1152 + j * 288;
        UNR for (int e = 0; e < 4; ++e) *(u32x4*)(od + e * 64 + q * 16) = u32x4{cvtpk(v[0][e], v[1][e]), cvtpk(v[2][e], v[3][e]), cvtpk(v[4][e], v[5][e]), cvtpk(v[6][e], v[7][e])};
        *(u32x2*)(od + 256 + q * 8) = u32x2{cvtpk(v[8][0], v[8][1]), cvtpk(v[8][2], v[8][3])};
    }
}

#define KT_FWD

__global__ __launch_bounds__(512, 2) void block_fwd(const char* __restrict__ x, const char* __restrict__ wqkv, const char* __restrict__ wo, const char* __restrict__ wg,
                                                    const char* __restrict__ wu, const char* __restrict__ wd, char* __restrict__ ws, char* __restrict__ out KT_ARG) {
    extern __shared__ __attribute__((aligned(16))) char smem[];
    const int w = rfl(threadIdx.x >> 6), xc = blockIdx.x & 7, rk = blockIdx.x >> 3, hs = rk & 3, qb = rk >> 2;
    KT_MARK(0);
    u32x2 ep; gl8c<0>(ep, WS_EPOCH, ws);
    const int mt1 = (xc >> 1) * 8 + (rk & 7), ct1 = (xc & 1) * 4 + (rk >> 3);
    phase1(x, wqkv, wo, ws, w, mt1, ct1 KT_FWD);
    asm volatile("s_waitcnt vmcnt(0) lgkmcnt(0)\n\ts_barrier" : "+v"(ep) :: "memory");
    KT_MARK(4);
    const u64 tag = (((u64)rfl(ep[1]) << 32) | rfl(ep[0])) + 1; const unsigned tlo = (unsigned)tag, thi = (unsigned)(tag >> 32);
    if (w == 0) { const int lane = lane_id(), pp = xc * 2 + (hs >> 1);
        if (lane < 2) set_flag(ws, lane ? WS_FD + blockIdx.x * 8 : WS_F1 + (ct1 * 32 + mt1) * 128, tlo, thi);
        poll_flags2(ws, WS_F1 + (((lane < 32 ? 16 : 32) + pp) / 6 * 32 + (lane & 31)) * 128, WS_F1 + (pp / 6 * 32 + qb * 4 + (lane & 3)) * 128, tlo, thi); }
    asm volatile("s_barrier" ::: "memory");
    KT_MARK(5);
    P3Pre P;
    const int mt4 = qb, fs4 = xc * 4 + hs, m5 = qb * 2 + (xc & 1), j5 = hs, s5 = xc >> 1;
    phase2(x, wg, wu, wd, ws, P, w, xc * 4 + hs, qb, qb * 8 + xc, hs, (m5 * 4 + j5) * 4 + s5, tlo, thi KT_FWD);
    if (w == 0) poll_dense(ws, WS_FR, lane_id(), tlo, thi);
    KT_MARK(8);
    fullbar();
    UNR for (int i = 0; i < 5; ++i) asm volatile("" : "+v"(P.wf[i][0]), "+v"(P.wf[i][1]), "+v"(P.wf[i][2]), "+v"(P.wf[i][3]), "+v"(P.wf[i][4]));
    asm volatile("" : "+v"(P.xa), "+v"(P.xb), "+v"(P.xm));
    KT_MARK(9);
    if (w == 0 && lane_id() == 0) set_flag(ws, WS_F2 + (qb * 32 + xc * 4 + hs) * 128, tlo, thi);
    WStream S; S.lv = lane_id() * 16; S.lw = w * 1024;
    S.a = ws + WS_WGU + fs4 * 324 * 1024 + w * 1024;
    S.b = ws + WS_WD + (j5 * 144 + s5 * 36) * 9216 + w * 1024 - 4096;
    sfor<9>([&](auto J) { wround<J>(S, w); });
    if (w == 0) poll_flags(ws, WS_F2 + (qb * 32 + (lane_id() & 31)) * 128, true, tlo, thi);
    asm volatile("s_barrier" ::: "memory");
    KT_MARK(10);
    phase3(ws, P, w, qb * 8 + xc, hs KT_FWD);
    fullbar();
    KT_MARK(12);
    if (w == 0 && lane_id() == 0) set_flag(ws, WS_F3 + (qb * 32 + xc * 4 + hs) * 128, tlo, thi);
    sfor<9>([&](auto J) { wround<9 + J>(S, w); });
    if (w == 0) poll_flags(ws, WS_F3 + (mt4 * 32 + (lane_id() & 31)) * 128, true, tlo, thi);
    asm volatile("s_barrier" ::: "memory");
    KT_MARK(13);
    phase4(ws, S, w, mt4 * 8 + w, fs4 KT_FWD);
    fullbar();
    KT_MARK(17);
    if (w == 0 && lane_id() == 0) set_flag(ws, WS_F4 + (mt4 * 32 + fs4) * 128, tlo, thi);
    sfor<K5_W0 - K4_WEND>([&](auto J) { wround<K4_WEND + J>(S, w); });
    if (w == 0) poll_flags(ws, WS_F4 + ((m5 >> 1) * 32 + s5 * 8 + (lane_id() & 7)) * 128, true, tlo, thi);
    asm volatile("s_barrier" ::: "memory");
    KT_MARK(18);
    phase5(ws, out, S, w, m5, j5, s5 KT_FWD);
    if (blockIdx.x == 0 && w == 0 && lane_id() == 0) set_flag(ws, WS_EPOCH, tlo, thi);
    KT_MARK(22);
}

#define KT_PASS

extern "C" void kernel_launch(void* const* d_in, const int* in_sizes, int n_in, void* d_out, int out_size, void* d_ws, size_t ws_size, hipStream_t stream) {
    (void)hipFuncSetAttribute((const void*)block_fwd, hipFuncAttributeMaxDynamicSharedMemorySize, LDS_BYTES);
    block_fwd<<<256, 512, LDS_BYTES, stream>>>((const char*)d_in[0], (const char*)d_in[1], (const char*)d_in[2], (const char*)d_in[4], (const char*)d_in[5], (const char*)d_in[6],
                                                (char*)d_ws, (char*)d_out KT_PASS);
}
```

```cpp
#define POLLSLEEP
#include <hip/hip_runtime.h>
#include <hip/hip_bf16.h>
#include <stdint.h>
#include <utility>

typedef float f32x16 __attribute__((ext_vector_type(16)));
typedef float f32x4 __attribute__((ext_vector_type(4)));
typedef float f32x2 __attribute__((ext_vector_type(2)));
typedef __bf16 bf16x8 __attribute__((ext_vector_type(8)));
typedef __bf16 bf16x2 __attribute__((ext_vector_type(2)));
typedef short s16x4 __attribute__((ext_vector_type(4)));
typedef unsigned u32x4 __attribute__((ext_vector_type(4)));
typedef unsigned u32x2 __attribute__((ext_vector_type(2)));
typedef unsigned long long u64;
#define LAS __attribute__((address_space(3)))
#define GAS __attribute__((address_space(1)))
#define DI __device__ __forceinline__
#define HD __host__ __device__
#define SBAR() __builtin_amdgcn_sched_barrier(0)
#define PIN(x) asm volatile("" : "+v"(x))
#define UNR _Pragma("unroll")
template <int V> using IC = std::integral_constant<int, V>;

#define ROT1(mt) ((mt) & 7)
constexpr unsigned QIMG = 2304, KVIMG = 4864;
constexpr unsigned WS_EPOCH = 0;
constexpr unsigned WS_FR = 256, WS_FD = WS_FR + 2048;
constexpr unsigned WS_F1 = 8192, WS_F2 = WS_F1 + 32768, WS_F3 = WS_F2 + 32768, WS_F4 = WS_F3 + 32768;
constexpr unsigned WS_XMEAN = WS_F4 + 32768;
constexpr unsigned WS_CM = WS_XMEAN + 8192;
constexpr unsigned WS_SSQ = WS_CM + 8192;
constexpr unsigned WS_WBAR = WS_SSQ + 32768;
constexpr unsigned WS_Q = WS_WBAR + 18432;
constexpr unsigned WS_KV = WS_Q + 32 * 32 * QIMG;
constexpr unsigned WS_O = WS_KV + 32 * 32 * KVIMG;
constexpr unsigned WS_OR = WS_O + 128 * 16 * 1024;
constexpr unsigned WS_HC = WS_OR + 64 * 32 * 128;
constexpr unsigned WS_WO = WS_HC + 64 * 36 * 1024;
constexpr unsigned WS_WGU = WS_WO + 4 * 18 * 9 * 1024;
constexpr unsigned WS_WD = WS_WGU + 32 * 324 * 1024;
constexpr unsigned WS_A = WS_WD + 4 * 144 * 9 * 1024;
constexpr unsigned WS_XB = WS_A + 128 * 144 * 1024;
constexpr unsigned WS_END = WS_XB + 256 * 32768;
constexpr int LDS_BYTES = 163840;

DI unsigned rfl(unsigned x) { return __builtin_amdgcn_readfirstlane(x); }
DI unsigned cvtpk(float lo, float hi) { f32x2 v = {lo, hi}; bf16x2 b = __builtin_convertvector(v, bf16x2); return __builtin_bit_cast(unsigned, b); }
DI float bflo(unsigned u) { return __uint_as_float(u << 16); }
DI float bfhi(unsigned u) { return __uint_as_float(u & 0xffff0000u); }
DI float dot2(unsigned a, unsigned b, float c) { return __builtin_amdgcn_fdot2_f32_bf16(__builtin_bit_cast(bf16x2, a), __builtin_bit_cast(bf16x2, b), c, false); }
DI u32x4 addc8(const u32x4& v, unsigned lo, unsigned hi, float c) { float t0, t1, t2, t3, t4, t5, t6, t7;
    asm("v_dot2_f32_bf16 %0, %8, %12, %14\n\tv_dot2_f32_bf16 %1, %8, %13, %14\n\tv_dot2_f32_bf16 %2, %9, %12, %14\n\tv_dot2_f32_bf16 %3, %9, %13, %14\n\t"
        "v_dot2_f32_bf16 %4, %10, %12, %14\n\tv_dot2_f32_bf16 %5, %10, %13, %14\n\tv_dot2_f32_bf16 %6, %11, %12, %14\n\tv_dot2_f32_bf16 %7, %11, %13, %14\n\ts_nop 2"
        : "=&v"(t0), "=&v"(t1), "=&v"(t2), "=&v"(t3), "=&v"(t4), "=&v"(t5), "=&v"(t6), "=&v"(t7) : "v"(v[0]), "v"(v[1]), "v"(v[2]), "v"(v[3]), "v"(lo), "v"(hi), "v"(c));
    return u32x4{cvtpk(t0, t1), cvtpk(t2, t3), cvtpk(t4, t5), cvtpk(t6, t7)}; }
DI f32x16 mfma32(u32x4 a, u32x4 b, f32x16 c) { return __builtin_amdgcn_mfma_f32_32x32x16_bf16(__builtin_bit_cast(bf16x8, a), __builtin_bit_cast(bf16x8, b), c, 0, 0, 0); }
DI f32x16 mfma32k8(u32x2 a, u32x2 b, f32x16 c) { return __builtin_amdgcn_mfma_f32_32x32x8bf16_1k(__builtin_bit_cast(s16x4, a), __builtin_bit_cast(s16x4, b), c, 0, 0, 0); }
DI f32x4 mfma16(u32x4 a, u32x4 b, f32x4 c) { return __builtin_amdgcn_mfma_f32_16x16x32_bf16(__builtin_bit_cast(bf16x8, a), __builtin_bit_cast(bf16x8, b), c, 0, 0, 0); }
DI void mfma16a(f32x4& c, const u32x4& a, const u32x4& b) { asm volatile("v_mfma_f32_16x16x32_bf16 %0, %1, %2, %0" : "+v"(c) : "v"(a), "v"(b)); }
DI void mfma16z(f32x4& c, const u32x4& a, const u32x4& b) { asm volatile("v_mfma_f32_16x16x32_bf16 %0, %1, %2, 0" : "=&v"(c) : "v"(a), "v"(b)); }
DI void glds16(unsigned voff, const void* sb, unsigned lds) {
    asm volatile("s_mov_b32 m0, %2\n\ts_nop 0\n\tglobal_load_lds_dwordx4 %0, %1" :: "v"(voff), "s"(sb), "s"(lds) : "memory"); }
DI void glds16c(unsigned voff, const void* sb, unsigned lds) {
    asm volatile("s_mov_b32 m0, %2\n\ts_nop 0\n\tglobal_load_lds_dwordx4 %0, %1 sc1" :: "v"(voff), "s"(sb), "s"(lds) : "memory"); }
template <int OFF> DI void gl16(u32x4& d, unsigned voff, const void* sb) { asm volatile("global_load_dwordx4 %0, %1, %2 offset:%3" : "=v"(d) : "v"(voff), "s"(sb), "i"(OFF) : "memory"); }
template <int OFF> DI void gl16c(u32x4& d, unsigned voff, const void* sb) { asm volatile("global_load_dwordx4 %0, %1, %2 offset:%3 sc1" : "=v"(d) : "v"(voff), "s"(sb), "i"(OFF) : "memory"); }
template <int OFF> DI void gl8c(u32x2& d, unsigned voff, const void* sb) { asm volatile("global_load_dwordx2 %0, %1, %2 offset:%3 sc1" : "=v"(d) : "v"(voff), "s"(sb), "i"(OFF) : "memory"); }
template <int OFF> DI void gl4c(unsigned& d, unsigned voff, const void* sb) { asm volatile("global_load_dword %0, %1, %2 offset:%3 sc1" : "=v"(d) : "v"(voff), "s"(sb), "i"(OFF) : "memory"); }
DI void gs16c(unsigned voff, void* sb, u32x4 v) { asm volatile("global_store_dwordx4 %0, %1, %2 sc1\n\ts_nop 1" :: "v"(voff), "v"(v), "s"(sb) : "memory"); }
DI void gs8c(unsigned voff, void* sb, u32x2 v) { asm volatile("global_store_dwordx2 %0, %1, %2 sc1" :: "v"(voff), "v"(v), "s"(sb) : "memory"); }
DI void gs4c(unsigned voff, void* sb, unsigned v) { asm volatile("global_store_dword %0, %1, %2 sc1" :: "v"(voff), "v"(v), "s"(sb) : "memory"); }
template <int N> DI void waitv() { asm volatile("s_waitcnt vmcnt(%0)" :: "i"(N) : "memory"); }
template <int N> DI void waitv_bar() { asm volatile("s_waitcnt vmcnt(%0)\n\ts_barrier" :: "i"(N) : "memory"); }
template <int N> DI void waitv_pin(u32x4& a) { asm volatile("s_waitcnt vmcnt(%1)" : "+v"(a) : "i"(N) : "memory"); }
DI void lbar() { asm volatile("s_waitcnt lgkmcnt(0)\n\ts_barrier" ::: "memory"); }
DI void fullbar() { asm volatile("s_waitcnt vmcnt(0) lgkmcnt(0)\n\ts_barrier" ::: "memory"); }
DI u32x4 lds128(unsigned a) { return *(const LAS u32x4*)(a); }
DI u32x2 lds64(unsigned a) { return *(const LAS u32x2*)(a); }
DI void sts128(unsigned a, u32x4 v) { *(LAS u32x4*)(a) = v; }
DI u32x2 ldstr(unsigned a) { return __builtin_bit_cast(u32x2, __builtin_amdgcn_ds_read_tr16_b64_v4i16((LAS s16x4*)(a))); }
template <int CTRL> DI float dpp_add(float v) { return v + __uint_as_float(__builtin_amdgcn_update_dpp(0u, __float_as_uint(v), CTRL, 0xf, 0xf, true)); }
DI float red8(float v) { v = dpp_add<0xB1>(v); v = dpp_add<0x4E>(v); return dpp_add<0x141>(v); }
DI float red16(float v) { v = red8(v); return dpp_add<0x140>(v); }
template <class F, int... Is> DI void sfor_impl(F&& f, std::integer_sequence<int, Is...>) { (f(IC<Is>{}), ...); }
template <int N, class F> DI void sfor(F&& f) { sfor_impl(f, std::make_integer_sequence<int, N>{}); }
DI int lane_id() { int l; asm volatile("v_mbcnt_lo_u32_b32 %0, -1, 0\n\tv_mbcnt_hi_u32_b32 %0, -1, %0" : "=v"(l)); return l; }

DI void set_flag(char* ws, unsigned off, unsigned tlo, unsigned thi) { gs8c(off, ws, u32x2{tlo, thi}); }
DI void poll_flags(const char* ws, unsigned off, bool need, unsigned tlo, unsigned thi) {
    for (unsigned spins = 0; spins < (1u << 20); ++spins) { u32x2 v; gl8c<0>(v, off, ws); asm volatile("s_waitcnt vmcnt(0)" : "+v"(v) :: "memory");
        if (__all(!need || (v[0] == tlo && v[1] == thi))) break; POLLSLEEP } }
DI void poll_flags2(const char* ws, unsigned o1, unsigned o2, unsigned tlo, unsigned thi) {
    for (unsigned spins = 0; spins < (1u << 20); ++spins) { u32x2 v, u; gl8c<0>(v, o1, ws); gl8c<0>(u, o2, ws); asm volatile("s_waitcnt vmcnt(0)" : "+v"(v), "+v"(u) :: "memory");
        if (__all(v[0] == tlo && v[1] == thi && u[0] == tlo && u[1] == thi)) break; POLLSLEEP } }
DI void poll_dense(const char* ws, unsigned base, int lane, unsigned tlo, unsigned thi) {
    for (unsigned spins = 0; spins < (1u << 20); ++spins) { u32x4 v, u; gl16c<0>(v, base + lane * 16, ws); gl16c<1024>(u, base + lane * 16, ws); asm volatile("s_waitcnt vmcnt(0)" : "+v"(v), "+v"(u) :: "memory");
        if (__all(v[0] == tlo && v[1] == thi && v[2] == tlo && v[3] == thi && u[0] == tlo && u[1] == thi && u[2] == tlo && u[3] == thi)) break; __builtin_amdgcn_s_sleep(4); } }

HD constexpr int vslot(int r) { return r < 4 ? r : r < 12 ? r + 6 : r < 16 ? r - 6 : r + 2; }
HD constexpr int k4pos(int t, int ks) { return t >= 3 ? 36 * t + ks : (ks < 24 ? 3 * ks + t : 72 + 12 * t + (ks - 24)); }

#define KT_ARG
#define KT_MARK(i)

struct Retile { u32x4 in[8]; unsigned e[8]; };
template <int J> DI void rt_load(Retile& R, const char* p, int lo, int hi) { const char* a = p + ((J & 3) * lo + (J >> 2) * hi);
    asm volatile("global_load_dwordx4 %0, %1, off" : "=v"(R.in[J]) : "v"(a) : "memory"); }
template <int J> DI void rt_load2(Retile& R, const char* p, int lo, int hi) { const char* a = p + ((J & 3) * lo + (J >> 2) * hi);
    asm volatile("global_load_ushort %0, %1, off" : "=v"(R.e[J]) : "v"(a) : "memory"); }
DI void rt_wait(Retile& R) { asm volatile("s_waitcnt vmcnt(0)" : "+v"(R.in[0]), "+v"(R.in[1]), "+v"(R.in[2]), "+v"(R.in[3]), "+v"(R.in[4]), "+v"(R.in[5]), "+v"(R.in[6]), "+v"(R.in[7]) :: "memory"); }
template <int I> DI u32x4 rt_col(const Retile& R) { u32x4 o;
    UNR for (int k = 0; k < 4; ++k) o[k] = __builtin_amdgcn_perm(R.in[2 * k + 1][I >> 1], R.in[2 * k][I >> 1], (I & 1) ? 0x07060302u : 0x05040100u); return o; }
DI u32x4 rt_col9(Retile& R) { asm volatile("" : "+v"(R.e[0]), "+v"(R.e[1]), "+v"(R.e[2]), "+v"(R.e[3]), "+v"(R.e[4]), "+v"(R.e[5]), "+v"(R.e[6]), "+v"(R.e[7]));
    return u32x4{R.e[0] | R.e[1] << 16, R.e[2] | R.e[3] << 16, R.e[4] | R.e[5] << 16, R.e[6] | R.e[7] << 16}; }

constexpr int K1_XB = 528, K1_W = 2 * 72 * K1_XB, K1_BLK = 7168, K1_RS = K1_W + 12 * K1_BLK;

DI int wrap36(int v) { return v >= 36 ? v - 36 : v; }
template <int MODE> DI void p1_loop(f32x16& c0, f32x16& c1, unsigned xa, unsigned xb, unsigned wa, const unsigned (&wv)[7], const char* wq, int w, int rot) {
    u32x4 wf[8], x0[2], x1[2];
    auto rw = [&](auto K) { constexpr int ks = K; const u32x2 w0 = ldstr(wa + (ks % 12) * K1_BLK), w1 = ldstr(wa + (ks % 12) * K1_BLK + 1792); wf[ks % 8] = u32x4{w0[0], w0[1], w1[0], w1[1]}; };
    auto rx = [&](auto K) { constexpr int ks = K; x0[ks & 1] = lds128(xa + ks * 2 * K1_XB); x1[ks & 1] = lds128(xb + ks * 2 * K1_XB); };
    if constexpr (MODE != 2) { sfor<4>(rw); rx(IC<0>{}); }
    sfor<9>([&](auto P) { constexpr int p = P;
        if constexpr (p < 8) { if ((w >> 2) == ((p + 1) & 1)) waitv<0>(); lbar(); }
        const bool own = p <= 5 && (w >> 2) == ((p + 1) & 1);
        const int blk = 4 * (p + 3) + (w & 3); const char* s = wq + wrap36(blk + rot) * (16 * 3456); const unsigned l = K1_W + (blk % 12) * K1_BLK;
        if constexpr (MODE == 2) { if (own) { UNR for (int i = 0; i < 7; ++i) glds16(wv[i], s, l + i * 1024); } }
        else sfor<4>([&](auto KK) { constexpr int ks = 4 * p + KK;
            if constexpr (ks + 1 < 36) rx(IC<ks + 1>{});
            if (own) glds16(wv[2 * KK], s, l + 2 * KK * 1024);
            if constexpr (MODE) c0 = mfma32(x0[ks & 1], wf[ks % 8], c0); else c0 = mfma32(wf[ks % 8], x0[ks & 1], c0);
            SBAR();
            if constexpr (ks + 4 < 36) rw(IC<ks + 4>{});
            if constexpr (KK < 3) { if (own) glds16(wv[2 * KK + 1], s, l + (2 * KK + 1) * 1024); }
            if constexpr (MODE) c1 = mfma32(x1[ks & 1], wf[ks % 8], c1); else c1 = mfma32(wf[ks % 8], x1[ks & 1], c1);
            SBAR(); });
    });
}

DI void phase1(const char* x, const char* wqkv, const char* wo, char* ws, int w, int mt, int ct KT_ARG) {
    const int lane = lane_id(), tid = w * 64 + lane;
    u32x4 xv[9];
    { const unsigned vo = (unsigned)((w * 8 + (lane >> 3)) * 1152 + (lane & 7) * 16); const char* xs = x + (size_t)mt * 64 * 1152;
      sfor<9>([&](auto I) { const int g9 = I + ROT1(mt); gl16<0>(xv[I], vo, xs + (g9 >= 9 ? g9 - 9 : g9) * 128); }); }
    const bool idle = (w == 7 && ct < 5) || (w == 6 && ct > 5); const int id = (ct < 5 ? ct : ct - 1) * 32 + mt; Retile R; u32x4 r9;
    if (idle && id < 144) { const bool fr = id < 72; const int j = id & 3, u = (fr ? id : id - 72) >> 2, rr = lane & 15, gq = fr ? lane >> 4 : j, oc = 4 * (rr & 3) + (rr >> 2), jr = fr ? 0 : lane >> 3;
        const int row = u < 16 ? 36 * u + (gq >> 1) * 20 + 4 * (gq & 1) + (jr & 3) + (jr >> 2) * 8 : 36 * (8 * (u - 16) + 2 * gq + (jr >> 2)) + 16 + (jr & 3);
        const char* p = wo + (row * 1152 + (fr ? j * 288 + oc * 16 : (lane & 7) * 16)); const int lo = fr ? 1152 : 128, hi = fr ? (u < 16 ? 9216 : 41472) : 512;
        sfor<8>([&](auto J) { rt_load<J>(R, p, lo, hi); rt_load2<J>(R, p + (fr ? 256 + rr * 2 - oc * 16 : 0), lo, hi); });
        asm volatile("global_load_dwordx4 %0, %1, off" : "=v"(r9) : "v"(p + (fr ? 0 : 1024)) : "memory"); }
    unsigned wv[7];
    UNR for (int i = 0; i < 7; ++i) { const int c = i * 64 + lane, row = (c * 2341) >> 16, ch = c - row * 28; wv[i] = (unsigned)(row * 3456 + (ch < 27 ? ch : 26) * 16); }
    const int rot = ROT1(mt) * 4; const char* wq = wqkv + ct * 432; const char* wq0 = wq + wrap36(w + rot) * (16 * 3456); const char* wq1 = wq + wrap36(w + 8 + rot) * (16 * 3456);
    asm volatile("s_barrier" ::: "memory");
    auto dm = [&](auto I) { constexpr int i = I; SBAR();
        if constexpr (i < 7) glds16(wv[i], wq0, K1_W + w * K1_BLK + i * 1024);
        else { if (w < 4) glds16(wv[i - 7], wq1, K1_W + (w + 8) * K1_BLK + (i - 7) * 1024); } SBAR(); };
    auto dm1 = [&](auto I) { if (w >= 4) dm(I); };
    if (w < 4) { sfor<7>(dm); waitv<7>(); } else { sfor<2>(dm); waitv<2>(); }
    asm volatile("" : "+v"(xv[0]), "+v"(xv[1]), "+v"(xv[2]), "+v"(xv[3]), "+v"(xv[4]), "+v"(xv[5]), "+v"(xv[6]), "+v"(xv[7]), "+v"(xv[8]));
    KT_MARK(1);
    float mean; const int xrow = w * 8 + (lane >> 3);
    { float s0 = 0, s1 = 0, s2 = 0, s3 = 0; unsigned onev = 0x3f803f80u; PIN(onev);
      sfor<9>([&](auto I) { constexpr int i = I; s0 = dot2(xv[i][0], onev, s0); s1 = dot2(xv[i][1], onev, s1); s2 = dot2(xv[i][2], onev, s2); s3 = dot2(xv[i][3], onev, s3);
          if constexpr (i == 4) { PIN(s0); dm1(IC<2>{}); } });
      mean = red8((s0 + s1) + (s2 + s3)) * (1.f / 576.f); }
    { const unsigned xa = ((xrow >> 5) * 72 + (lane & 7)) * K1_XB + (xrow & 31) * 16;
      unsigned slo = 0x00003f80u, shi = 0x3f800000u; float nm = -mean; PIN(slo); PIN(shi); PIN(nm);
      sfor<9>([&](auto I) { constexpr int i = I; xv[i] = addc8(xv[i], slo, shi, nm); sts128(xa + i * 8 * K1_XB, xv[i]); if constexpr (i % 2 == 0 && i < 8) dm1(IC<3 + i / 2>{}); }); }
    if (w < 4) waitv<0>();
    lbar();
    KT_MARK(2);
    { float q0 = 0, q1 = 0, q2 = 0, q3 = 0;
      sfor<9>([&](auto I) { constexpr int i = I; q0 = dot2(xv[i][0], xv[i][0], q0); q1 = dot2(xv[i][1], xv[i][1], q1); q2 = dot2(xv[i][2], xv[i][2], q2); q3 = dot2(xv[i][3], xv[i][3], q3);
          if constexpr (i < 7) { PIN(q0); dm(IC<7 + i>{}); } });
      const float rstd = __builtin_amdgcn_rsqf(red8((q0 + q1) + (q2 + q3)) * (1.f / 576.f) + 1e-5f);
      if ((lane & 7) == 0) *(LAS float*)(K1_RS + xrow * 4) = rstd; }
    const int r = lane & 31, hh = lane >> 5;
    unsigned xa = hh * K1_XB + r * 16, xb = xa + 72 * K1_XB, wa; PIN(xa); PIN(xb);
    { const int cg = (lane >> 4) & 1, q = (lane & 15) >> 2, p = lane & 3;
      wa = K1_W + (8 * hh + q) * 448 + (w < 6 ? 72 * w + cg * 40 + 8 * p : 32 + 72 * (cg ? 4 + (p & 1) : p)); }
    f32x16 c0 = {}, c1 = {};
    const int gp = 6 * ct + w;
    const bool vor = w < 6 ? gp >= 32 : w == 7;
    if (idle) {
        p1_loop<2>(c0, c1, xa, xb, wa, wv, wq, w, rot);
        if (id < 144) { asm volatile("" : "+v"(R.in[0]), "+v"(R.in[1]), "+v"(R.in[2]), "+v"(R.in[3]), "+v"(R.in[4]), "+v"(R.in[5]), "+v"(R.in[6]), "+v"(R.in[7]), "+v"(r9));
            if (id < 72) { char* d = ws + WS_WO + ((id & 3) * 18 + (id >> 2)) * 9216;
                sfor<8>([&](auto I) { gs16c(lane * 16, d + I * 1024, rt_col<I>(R)); }); gs16c(lane * 16, d + 8192, rt_col9(R));
            } else { float s = 0.f; UNR for (int c = 0; c < 4; ++c) { s += bflo(r9[c]) + bfhi(r9[c]); UNR for (int i = 0; i < 8; ++i) s += bflo(R.in[i][c]) + bfhi(R.in[i][c]); }
                s = red8(s) * (1.f / 576.f);
                const unsigned hb = cvtpk(s, s) & 0xffffu; const float rest = s - bflo(hb); const unsigned both = hb | (cvtpk(rest, rest) & 0xffff0000u); u32x4 o;
                UNR for (int c = 0; c < 4; ++c) { const unsigned a = __builtin_amdgcn_readlane(both, 16 * c), b = __builtin_amdgcn_readlane(both, 16 * c + 8);
                    const unsigned v0 = (a & 0xffffu) | b << 16, v1 = (a >> 16) | (b & 0xffff0000u); o[c] = lane == 0 ? v0 : lane == 1 ? v1 : 0u; }
                if (lane < 16) gs16c(WS_WBAR + (id - 72) * 256 + lane * 16, ws, o); } }
    } else if (vor) p1_loop<1>(c0, c1, xa, xb, wa, wv, wq, w, rot); else p1_loop<0>(c0, c1, xa, xb, wa, wv, wq, w, rot);
    KT_MARK(3);
    constexpr float QS = 0.23570226039551584f * 1.4426950408889634f;
    constexpr unsigned st = K1_W, HB = 2560;
#define PK8(c, o, f) u32x4{cvtpk(c[o] * f, c[o + 1] * f), cvtpk(c[o + 2] * f, c[o + 3] * f), cvtpk(c[o + 4] * f, c[o + 5] * f), cvtpk(c[o + 6] * f, c[o + 7] * f)}
    if (!idle) {
        if (!vor) { const float r0 = *(const LAS float*)(K1_RS + r * 4), r1 = *(const LAS float*)(K1_RS + 128 + r * 4);
            if (w < 6) { const float f = gp < 16 ? QS : 1.f, f0 = f * r0, f1 = f * r1; const unsigned hb = st + 2 * w * HB + lane * 16;
                sts128(hb, PK8(c0, 0, f0)); sts128(hb + 1024, PK8(c1, 0, f1)); sts128(hb + HB, PK8(c0, 8, f0)); sts128(hb + HB + 1024, PK8(c1, 8, f1));
            } else { UNR for (int i = 0; i < 3; ++i) { const int pl = 2 * i + hh, g2 = 6 * ct + pl; const float f = g2 < 16 ? QS : 1.f, f0 = f * r0, f1 = f * r1;
                    UNR for (int e = 0; e < 2; ++e) { const unsigned hb = st + (2 * pl + e) * HB + 2048;
                        const unsigned v0 = cvtpk(c0[4 * i + 2 * e] * f0, c0[4 * i + 2 * e + 1] * f0), v1 = cvtpk(c1[4 * i + 2 * e] * f1, c1[4 * i + 2 * e + 1] * f1);
                        if (g2 < 32) { *(LAS unsigned*)(hb + (g2 < 16 ? r * 4 : r * 8)) = v0; *(LAS unsigned*)(hb + (g2 < 16 ? 128 + r * 4 : r * 8 + 4)) = v1; } } } }
        } else { const int hd = w < 6 ? 2 * w + (r >> 4) : r >> 1, rho = w < 6 ? r & 15 : 16 + (r & 1);
            UNR for (int i = 0; i < 4; ++i) { const f32x4 r0 = *(const LAS f32x4*)(K1_RS + i * 32 + hh * 16), r1 = *(const LAS f32x4*)(K1_RS + 128 + i * 32 + hh * 16);
                UNR for (int e = 0; e < 4; ++e) { c0[4 * i + e] *= r0[e]; c1[4 * i + e] *= r1[e]; } }
            if (w < 6 || (r < 24 && 6 * ct + (r >> 2) >= 32)) { const unsigned hb = st + hd * HB + hh * 320 + vslot(rho) * 16;
                sts128(hb, PK8(c0, 0, 1.f)); sts128(hb + 640, PK8(c0, 8, 1.f)); sts128(hb + 1280, PK8(c1, 0, 1.f)); sts128(hb + 1920, PK8(c1, 8, 1.f)); }
            if (w < 6 && r < 16) { const unsigned cv = hh ? 0u : 0x3f803f80u; sts128(st + 2 * w * HB + r * 320 + 64 + hh * 16, u32x4{cv, cv, cv, cv}); } } }
#undef PK8
    if (ct == 0 && (lane & 7) == 0) gs4c(WS_XMEAN + (mt * 64 + xrow) * 4, ws, __float_as_uint(mean));
    lbar();
    UNR for (int i = 0; i < 4; ++i) { const int u = tid + 512 * i, hd = (u * 6554) >> 20, o = (u - hd * 160) * 16, gh = 12 * ct + hd, sec = gh >> 5, hm = (gh & 31) * 32 + mt;
        if (u < 1920 && (sec == 2 || o < 2304)) gs16c(sec == 0 ? WS_Q + hm * QIMG + o : WS_KV + hm * KVIMG + (sec == 2 ? 2304 : 0) + o, ws, lds128(st + u * 16)); }
}

constexpr int K2_IMG = 32 * KVIMG, K2_HALF = 16 * KVIMG;
struct P3Pre { u32x4 wf[5][5], xa, xb; unsigned xm; };

DI float xhalf_max(float m) { auto rr = __builtin_amdgcn_permlane32_swap(__float_as_uint(m), __float_as_uint(m), false, false);
    return __builtin_fmaxf(__uint_as_float(rr[0]), __uint_as_float(rr[1])); }
DI float max16(const f32x16& s) { float a = __builtin_fmaxf(__builtin_fmaxf(s[0], s[1]), s[2]), b = __builtin_fmaxf(__builtin_fmaxf(s[3], s[4]), s[5]);
    a = __builtin_fmaxf(__builtin_fmaxf(a, s[6]), s[7]); b = __builtin_fmaxf(__builtin_fmaxf(b, s[8]), s[9]); a = __builtin_fmaxf(__builtin_fmaxf(a, s[10]), s[11]);
    b = __builtin_fmaxf(__builtin_fmaxf(b, s[12]), s[13]); a = __builtin_fmaxf(__builtin_fmaxf(a, s[14]), s[15]); return __builtin_fmaxf(a, b); }

struct RtTask { const char* rp; int reo; unsigned rd[8]; };
DI void rt_setup(RtTask& T, const char* wg, const char* wu, const char* wd, int gw) {
    const int lane = lane_id(); const bool isgu = gw < 1296, isd = !isgu && gw < 1872;
    const int span = gw / 36, ks = gw - span * 36, id = isd ? gw - 1296 : 0, rr = lane & 15, gq = lane >> 4, oc = 4 * (rr & 3) + (rr >> 2);
    const int u = id >> 2, s4 = (u * 1821) >> 16, ul = u - 36 * s4, TT = 9 * (8 * s4 + 2 * (ul & 3) + (gq >> 1)) + (ul >> 2);
    T.rp = isgu ? ((ks & 1) ? wu : wg) + ((32 * (ks >> 1) + 8 * gq) * 9216 + span * 256 + rr * 16) : wd + ((128 * (TT >> 3) + (TT & 7) + 32 * (gq & 1)) * 1152 + (id & 3) * 288 + oc * 16);
    T.reo = 256 + rr * 2 - oc * 16; PIN(T.rp); PIN(T.reo);
    const int pa = ks < 24 ? 3 * ks : 48 + ks, pm = ks < 24 ? 1 : 12;
    UNR for (int i = 0; i < 8; ++i) { const int T8 = 8 * span + i, fs = (T8 * 7282) >> 16, t = T8 - fs * 9;
        T.rd[i] = isgu ? WS_WGU + (fs * 324 + (t >= 3 ? 36 * t + ks : pa + pm * t)) * 1024 : WS_WD + ((id & 3) * 144 + (id >> 2)) * 9216 + i * 1024; asm volatile("" : "+s"(T.rd[i])); } }

DI void phase2(const char* x, const RtTask& T, char* ws, P3Pre& P, int w, int head, int qb, int t3, int j3, int xb5, unsigned tlo, unsigned thi KT_ARG) {
    const int lane = lane_id(), r = lane & 31, hh = lane >> 5, kh = w >> 2, gw = blockIdx.x * 8 + w;
    const unsigned lv = lane * 16, lk = kh * K2_HALF + (w & 3) * 1024;
    u32x4 qa, qb4; unsigned qra, qrb;
    { const char* qs = ws + WS_Q + (head * 32 + qb * 4 + (w & 3)) * QIMG; gl16c<0>(qa, lv, qs); gl16c<1024>(qb4, lv, qs); gl4c<2048>(qra, r * 4, qs); gl4c<2176>(qrb, r * 4, qs); }
    const char* kv = ws + WS_KV + head * K2_IMG + lk;
    UNR for (int j = 0; j < 3; ++j) glds16c(lv, kv + j * 4096, lk + j * 4096);
    unsigned ka = kh * K2_HALF + lv, kra = kh * K2_HALF + (hh ? 2304 + 64 : 2048 + r * 8), va = kh * K2_HALF + 2304 + hh * 320 + (r < 18 ? vslot(r) : (r == 18 || r == 22) ? 4 : 5) * 16;
    if (w == 0 && lane == 0) *(LAS unsigned*)(K2_IMG + 7680) = 0u;
    const bool isgu = gw < 1296, isd = !isgu && gw < 1872; const char* rp = T.rp; const int rhi = isgu ? 36864 : 73728, reo = T.reo;
    if (w >= 4) __builtin_amdgcn_s_setprio(1);
    asm volatile("s_waitcnt vmcnt(0) lgkmcnt(0)\n\ts_barrier" : "+v"(qa), "+v"(qb4), "+v"(qra), "+v"(qrb) :: "memory");
    KT_MARK(6);
    const f32x16 z = {};
    f32x16 oa = {}, ob = {}, sa, sb; u32x4 p0, p1 = {0u, 0u, 0u, 0u}, kf = lds128(ka), vf0, vf1 = {0u, 0u, 0u, 0u}; u32x2 kr = lds64(kra), va0, va1, vb0, vb1; float ma, mb;
    { const u32x2 ta = {hh ? 0u : qra, 0u}, tb = {hh ? 0u : qrb, 0u};
      sa = mfma32(kf, qa, z); sb = mfma32(kf, qb4, z); sa = mfma32k8(kr, ta, sa); sb = mfma32k8(kr, tb, sb);
      UNR for (int j = 3; j < 7; ++j) glds16c(lv, kv + j * 4096, lk + j * 4096);
      const unsigned mp = cvtpk(-xhalf_max(max16(sa)), -xhalf_max(max16(sb)));
      ma = -bflo(mp); mb = -bfhi(mp);
      va0 = u32x2{hh ? mp & 0xffffu : qra, 0u}; va1 = u32x2{hh ? mp & 0xffffu : 0u, hh ? 0u : qra}; vb0 = u32x2{hh ? mp >> 16 : qrb, 0u}; vb1 = u32x2{hh ? mp >> 16 : 0u, hh ? 0u : qrb};
      UNR for (int i = 0; i < 16; ++i) sa[i] -= ma; }
#define EX(v) __builtin_amdgcn_exp2f(v)
#define QUAD(S, i, P, j) do { const float e0 = EX(S[i]), e1 = EX(S[i + 1]), e2 = EX(S[i + 2]), e3 = EX(S[i + 3]); P[j] = cvtpk(e0, e1); P[j + 1] = cvtpk(e2, e3); PIN(P); SBAR(); } while (0)
#define STEP(SC, SN, QN, VN, OP, OC, LD, KOFF, VOFF, LDKR, H, H0) do { \
        if (LD) { vf0 = lds128(va + (VOFF)); SBAR(); } \
        SN = mfma32(kf, QN, z); SBAR(); if (LD) kf = lds128(ka + (KOFF)); QUAD(SC, 0, p0, 0); \
        SN = mfma32k8(kr, VN, SN); SBAR(); if (LDKR) kr = lds64(kra + KVIMG); H(IC<H0>{}); QUAD(SC, 4, p0, 2); \
        OP = mfma32(vf1, p1, OP); SBAR(); if (LD) vf1 = lds128(va + (VOFF) + 640); H(IC<H0 + 1>{}); QUAD(SC, 8, p1, 0); \
        OC = mfma32(vf0, p0, OC); SBAR(); H(IC<H0 + 2>{}); QUAD(SC, 12, p1, 2); } while (0)
#define GROUP(H) do { \
        STEP(sa, sb, qb4, vb0, ob, oa, true, 1024, 0, false, H, 0); STEP(sb, sa, qa, va1, oa, ob, false, 0, 0, false, H, 3); \
        STEP(sa, sb, qb4, vb1, ob, oa, true, KVIMG, 1280, true, H, 6); STEP(sb, sa, qa, va0, oa, ob, false, 0, 0, false, H, 9); \
        ka += KVIMG; va += KVIMG; kra += KVIMG; PIN(ka); PIN(va); PIN(kra); } while (0)
    auto none = [&](auto) {};
    Retile R;
    { auto h = [&](auto I) { glds16c(lv, kv + (7 + I) * 4096, lk + (7 + I) * 4096); }; GROUP(h); }
    if (w == 4) poll_dense(ws, WS_FD, lane, tlo, thi);
    { auto h = [&](auto I) { if constexpr (I < 8) { rt_load<I>(R, rp, 9216, rhi); if (isd) rt_load2<I>(R, rp + reo, 9216, rhi); } }; GROUP(h); }
    if (isd) waitv<16>(); else waitv<8>();
    asm volatile("s_barrier" ::: "memory");
    _Pragma("nounroll") for (int gi = 2; gi < 8; ++gi) GROUP(none);
    rt_wait(R);
    { auto h = [&](auto I) { if constexpr (I < 8) { if (isgu || isd) gs16c(lv, ws + T.rd[I], rt_col<I>(R)); } if constexpr (I == 8) { if (isd) gs16c(lv, ws + T.rd[7] + 1024, rt_col9(R)); } }; GROUP(h); }
    { const int kg = w & 3, ng = w >> 2, u0 = kg * 4 + (kg < 2 ? kg : 2), tid = w * 64 + lane;
      const char* wsrc = ws + WS_WO + ((j3 * 18 + u0) * 9 + ng * 4) * 1024; const char* w5 = ng ? ws + WS_WBAR + u0 * 1024 : wsrc + 8192; const int s5 = ng ? 1024 : 9216;
      auto ld = [&](auto N) { constexpr int n = N, i = n / 5, t = n % 5; const int ii = (i == 4 && kg >= 2) ? 3 : i;
          if constexpr (n < 25) { if constexpr (t < 4) gl16c<t * 1024>(P.wf[i][t], lv, wsrc + ii * 9216); else gl16c<0>(P.wf[i][4], lv, w5 + ii * s5); }
          if constexpr (n == 25) gl16<0>(P.xa, r * 1152 + (tid >> 5) * 16, x + (size_t)t3 * 32 * 1152 + j3 * 288);
          if constexpr (n == 26) gl16<256>(P.xb, r * 1152 + hh * 16, x + (size_t)t3 * 32 * 1152 + j3 * 288);
          if constexpr (n == 27) gl4c<0>(P.xm, r * 4, ws + WS_XMEAN + t3 * 128); };
      { auto h = [&](auto I) { ld(I); }; GROUP(h); }
      { auto h = [&](auto I) { ld(IC<12 + I>{}); }; GROUP(h); }
      { waitv<24>(); unsigned old = 0; if (lane == 0) old = __hip_atomic_fetch_add((LAS unsigned*)(K2_IMG + 7680), 1u, __ATOMIC_RELAXED, __HIP_MEMORY_SCOPE_WORKGROUP);
        if (lane == 0 && old == 7u) set_flag(ws, WS_FR + blockIdx.x * 8, tlo, thi); }
#define PRIOG 11
      if (PRIOG == 11) __builtin_amdgcn_s_setprio(0);
      { auto h = [&](auto I) { if constexpr (I < 4) ld(IC<24 + I>{});
                              else if constexpr (I % 2 == 0) gs16c(lv, ws + WS_XB + xb5 * 32768 + (w * 4 + I / 2 - 2) * 1024, u32x4{~0u, ~0u, ~0u, ~0u}); }; GROUP(h); } }
    if (PRIOG == 12) __builtin_amdgcn_s_setprio(0);
    _Pragma("nounroll") for (int gi = 12; gi < 16; ++gi) GROUP(none);
    ob = mfma32(vf1, p1, ob);
    KT_MARK(7);
    { bool bad = !(oa[10] < 1e12f && oa[10] > 1e-6f && ob[10] < 1e12f && ob[10] > 1e-6f);
      UNR for (int i = 0; i < 10; ++i) bad |= !(__builtin_fabsf(oa[i]) < 3e38f && __builtin_fabsf(ob[i]) < 3e38f);
      if (__builtin_expect(__any(bad), 0)) { ka -= K2_HALF; va -= K2_HALF; kra -= K2_HALF; const char* img = ws + WS_KV + head * K2_IMG;
        auto sc = [&](int t, const u32x4& q, const u32x2& t0, const u32x2& t1) { const unsigned cb = (t >> 1) * KVIMG; u32x4 k1; u32x2 k2; gl16c<0>(k1, ka + cb + (t & 1) * 1024, img); gl8c<0>(k2, kra + cb, img);
            asm volatile("s_waitcnt vmcnt(0)" : "+v"(k1), "+v"(k2) :: "memory"); return mfma32k8(k2, (t & 1) ? t1 : t0, mfma32(k1, q, z)); };
        auto redo = [&](const u32x4& q, unsigned q2, f32x16& o, float& m) { const u32x2 t0 = {q2, 0u}, t1 = {0u, q2}; m = -__builtin_inff();
            for (int t = 0; t < 32; ++t) m = __builtin_fmaxf(m, max16(sc(t, q, t0, t1)));
            m = xhalf_max(m); o = z;
            for (int t = 0; t < 32; ++t) { const unsigned cb = (t >> 1) * KVIMG + (t & 1) * 1280; u32x4 v0, v1, a, b; gl16c<0>(v0, va + cb, img); gl16c<640>(v1, va + cb, img);
                const f32x16 s = sc(t, q, t0, t1); asm volatile("" : "+v"(v0), "+v"(v1));
                UNR for (int i = 0; i < 4; ++i) { a[i] = cvtpk(EX(s[2 * i] - m), EX(s[2 * i + 1] - m)); b[i] = cvtpk(EX(s[8 + 2 * i] - m), EX(s[9 + 2 * i] - m)); }
                o = mfma32(v0, a, o); o = mfma32(v1, b, o); } };
        redo(qa, hh ? 0u : va0[0], oa, ma); redo(qb4, hh ? 0u : vb0[0], ob, mb); } }
    auto box = [&](int v) { return (unsigned)((v & 3) < 3 ? (v >> 2) * K2_HALF + (v & 3) * 3072 : K2_IMG + 1024 + (v >> 2) * 3072) + lv; };
    float o[11], mo = kh ? mb : ma;
    { const float ms = kh ? ma : mb; f32x4 sv[3];
      UNR for (int i = 0; i < 11; ++i) { sv[i >> 2][i & 3] = kh ? oa[i] : ob[i]; o[i] = kh ? ob[i] : oa[i]; } sv[2][3] = ms;
      UNR for (int i = 0; i < 3; ++i) *(LAS f32x4*)(box(w) + i * 1024) = sv[i]; }
    lbar();
    { f32x4 rv[3]; UNR for (int i = 0; i < 3; ++i) rv[i] = *(const LAS f32x4*)(box(w ^ 4) + i * 1024);
      const float M = __builtin_fmaxf(mo, rv[2][3]), f0 = EX(mo - M), f1 = EX(rv[2][3] - M);
      UNR for (int i = 0; i < 11; ++i) o[i] = o[i] * f0 + rv[i >> 2][i & 3] * f1; }
    const float il = __builtin_amdgcn_rcpf(o[10]); const int tt = qb * 8 + (w & 3) * 2 + kh;
    gs16c(WS_O + ((tt * 2 + (r >> 4)) * 16 + (head >> 1)) * 1024 + (2 * (head & 1) + hh) * 256 + (r & 15) * 16, ws,
          u32x4{cvtpk(o[0] * il, o[1] * il), cvtpk(o[2] * il, o[3] * il), cvtpk(o[4] * il, o[5] * il), cvtpk(o[6] * il, o[7] * il)});
    if (hh == 0) gs4c(WS_OR + (tt * 32 + head) * 128 + r * 4, ws, cvtpk(o[8] * il, o[9] * il));
#undef STEP
#undef GROUP
#undef QUAD
}

struct WStream { const char* a; const char* b; unsigned lv, lw; };
template <int J> DI void wround(const WStream& S, int w) {
    const char* p = J < 40 ? S.a + J * 8192 : J > 40 ? S.b + (J - 40) * 8192 : (w < 4 ? S.a + J * 8192 : S.b);
    glds16c(S.lv, p, S.lw + (J % 18) * 8192); }

constexpr int K3_ROW = 164 * 4, K3_L = LDS_BYTES - 4 * 32 * K3_ROW;
DI void phase3(char* ws, P3Pre& P, int w, int tt, int j KT_ARG) {
    const int lane = lane_id(), kg = w & 3, ng = w >> 2;
    const unsigned lv = lane * 16;
    const int u0 = kg * 4 + (kg < 2 ? kg : 2);
    u32x4 of[5][2]; unsigned t[16]; f32x4 acc[5][2] = {};
    { const char* os = ws + WS_O + (tt * 32 + u0) * 1024; const int lim = kg < 2 ? 4 : kg == 2 ? 3 : 1;
      if (kg == 3) { const char* rs = ws + WS_OR + tt * 4096; const unsigned ro = (lane >> 4) * 512 + (lane & 15) * 4;
          sfor<16>([&](auto I) { constexpr int i = I; gl4c<(i >> 3) * 2048 + ((i >> 2) & 1) * 64 + (i & 3) * 128>(t[i], ro, rs); }); }
      sfor<5>([&](auto I) { constexpr int i = I; const int ii = i < lim ? i : lim; gl16c<0>(of[i][0], lv, os + ii * 1024); gl16c<0>(of[i][1], lv, os + ii * 1024 + 16384); }); }
    sfor<5>([&](auto I) { constexpr int i = I;
        asm volatile("s_waitcnt vmcnt(%2)" : "+v"(of[i][0]), "+v"(of[i][1]) : "i"(8 - 2 * i) : "memory");
        if constexpr (i == 2 || i == 3) { if (kg == 3) { constexpr int b = (i - 2) * 8;
            asm volatile("" : "+v"(t[b]), "+v"(t[b + 1]), "+v"(t[b + 2]), "+v"(t[b + 3]), "+v"(t[b + 4]), "+v"(t[b + 5]), "+v"(t[b + 6]), "+v"(t[b + 7]));
            of[i][0] = u32x4{t[b], t[b + 1], t[b + 2], t[b + 3]}; of[i][1] = u32x4{t[b + 4], t[b + 5], t[b + 6], t[b + 7]}; } }
        if (i < 4 || kg < 2) { UNR for (int c = 0; c < 5; ++c) { acc[c][0] = mfma16(P.wf[i][c], of[i][0], acc[c][0]); acc[c][1] = mfma16(P.wf[i][c], of[i][1], acc[c][1]); } }
        SBAR(); });
    KT_MARK(11);
    { const int q = lane >> 4;
      UNR for (int b = 0; b < 2; ++b) { const unsigned rowb = K3_L + (kg * 32 + b * 16 + (lane & 15)) * K3_ROW;
          UNR for (int e = 0; e < 4; ++e) *(LAS f32x4*)(rowb + (32 * e + 8 * q + 4 * ng) * 4) = f32x4{acc[0][b][e], acc[1][b][e], acc[2][b][e], acc[3][b][e]};
          *(LAS f32x4*)(rowb + (128 + ng * 16 + 4 * q) * 4) = acc[4][b]; } }
    lbar();
    const unsigned rb = K3_L + (lane & 31) * K3_ROW; float cm = __uint_as_float(P.xm);
    UNR for (int k = 0; k < 4; ++k) { const f32x2 m2 = *(const LAS f32x2*)(rb + k * 32 * K3_ROW + 576); cm += m2[0] + m2[1]; }
    float sq = 0.f;
    auto unit = [&](unsigned col, const u32x4& xr, unsigned dst) { f32x4 a = {}, b = {};
        UNR for (int k = 0; k < 4; ++k) { a += *(const LAS f32x4*)(rb + k * 32 * K3_ROW + col); b += *(const LAS f32x4*)(rb + k * 32 * K3_ROW + col + 16); }
        const u32x4 hv = {cvtpk(a[0] + bflo(xr[0]) - cm, a[1] + bfhi(xr[0]) - cm), cvtpk(a[2] + bflo(xr[1]) - cm, a[3] + bfhi(xr[1]) - cm),
                          cvtpk(b[0] + bflo(xr[2]) - cm, b[1] + bfhi(xr[2]) - cm), cvtpk(b[2] + bflo(xr[3]) - cm, b[3] + bfhi(xr[3]) - cm)};
        gs16c(dst, ws, hv);
        UNR for (int e = 0; e < 4; ++e) { sq = fmaf(bflo(hv[e]), bflo(hv[e]), sq); sq = fmaf(bfhi(hv[e]), bfhi(hv[e]), sq); } };
    const unsigned dst = WS_HC + (tt * 2 + ((lane >> 4) & 1)) * 18432 + (18 * j + (lane >> 5)) * 256 + (lane & 15) * 16;
    unit((2 * w + (lane >> 5)) * 32, P.xa, dst + w * 512);
    if (w == 7) unit(512 + (lane >> 5) * 32, P.xb, dst + 4096);
    *(LAS float*)(73728 + (w * 64 + lane) * 4) = sq;
    if (w == 6 && j == 0 && lane < 32) gs4c(WS_CM + tt * 128 + lane * 4, ws, __float_as_uint(cm));
    lbar();
    if (w == 5 && lane < 32) { float t2 = 0.f; UNR for (int i = 0; i < 16; ++i) t2 += *(const LAS float*)(73728 + i * 128 + lane * 4);
        gs4c(WS_SSQ + (tt * 4 + j) * 128 + lane * 4, ws, __float_as_uint(t2)); }
}

constexpr int FPF = 5, K4_X0 = 4, K5_AW = 4, NSLOT = 486;
HD constexpr int k4_tile(int q) { return q < 72 ? q % 3 : q < 108 ? (q - 72) / 12 : q / 36; }
HD constexpr int k4_ks(int q) { return q < 72 ? q / 3 : q < 108 ? 24 + (q - 72) % 12 : q % 36; }
HD constexpr bool f_bar(int g) { return g < 324 ? (g > 0 && g % 24 == 0) : (g - 324) % 18 == 0; }
struct FSched { int wn[NSLOT + 1], ln[NSLOT + 1], wslot[81], lslot[72]; };
HD constexpr FSched f_make() { FSched F{}; int wn = 18, ln = K4_X0, cons = 0;
    for (int j = 0; j < 18; ++j) F.wslot[j] = -2; for (int i = 0; i < K4_X0; ++i) F.lslot[i] = -1;
    for (int g = 0; g < NSLOT; ++g) { F.wn[g] = wn; F.ln[g] = ln;
        if (f_bar(g)) cons = g < 324 ? g : 324 + 2 * (g - 324);
        if ((g < 324 ? (g >= 60 && g % 4 == 2) : g % 3 == 1) && wn < 81 && 8 * wn + 8 <= cons + 144) F.wslot[wn++] = g;
        if (g % 2 == 1) { if (ln < 36) F.lslot[ln++] = g;
            else if (ln < 72 && g > 312) { const int pp = (ln - 36) / 2, cur = g < 324 ? 0 : (g - 324) / 9;
                if (pp < cur + K5_AW && (pp < 10 || g >= 378) && (pp < 14 || g >= 414)) F.lslot[ln++] = g; } } }
    F.wn[NSLOT] = wn; F.ln[NSLOT] = ln; return F; }
constexpr FSched FS = f_make();
HD constexpr int f_after(int slot, int g) { return (FS.wn[g] - FS.wn[slot + 1]) + (FS.ln[g] - FS.ln[slot + 1]); }
HD constexpr int k4_epi_start(int e) { return e == 0 ? 86 : e == 8 ? 327 : 108 + 31 * (e - 1); }
HD constexpr int k4_epi_len(int e) { return e == 0 ? 22 : e == 7 ? 30 : e == 8 ? 18 : 31; }
HD constexpr int k4_epi_first(int e, int gap) { const int g = gap - 2 * k4_epi_start(e), n = 2 * k4_epi_len(e); return g <= 0 ? 0 : g >= n ? 70 : (g * 70 + n - 1) / n; }
HD constexpr int f_barwait(int g) {
    int nb = g + 1; while (nb < NSLOT && !f_bar(nb)) ++nb;
    const int lim = nb < 324 ? nb + FPF : 324 + 2 * (nb - 324) + 18; int need = (lim + 7) / 8 - 1; if (need > 80) need = 80;
    if (need >= FS.wn[g]) return -1;
    int n = (FS.wn[g] - need - 1) + (FS.ln[g] - (need < 18 ? 0 : FS.ln[FS.wslot[need]]));
    const int e = g == 264 ? 4 : g == 324 ? 6 : g == 360 ? 8 : -1;
    if (e >= 0) { const int m = f_after(k4_epi_start(e) + k4_epi_len(e) - 1, g); if (m < n) n = m; }
    return n > 63 ? 63 : n; }

DI void phase45(char* ws, char* out, const WStream& S, int w, int qb, int fs, int m128, int j, int s4, unsigned tlo, unsigned thi KT_ARG) {
    const int lane = lane_id(), tt = qb * 8 + w, tq = w & 3, kg = w >> 2;
    const unsigned lv = lane * 16;
    unsigned sq[8]; u32x4 xf[36], af[K5_AW][2];
    const char* xs = ws + WS_HC + tt * 36 * 1024;
    const char* as = ws + WS_A + ((m128 * 8 + tq * 2) * 144 + s4 * 36 + kg) * 1024;
    auto ldr = [&](auto K) { constexpr int k = K;
        if constexpr (k < 36) gl16c<((k >> 1) % 3) * 1024>(xf[k], lv, xs + ((k & 1) * 18 + (k >> 1) / 3 * 3) * 1024);
        else { constexpr int i = k - 36, p = i / 2; gl16c<0>(af[p % K5_AW][i & 1], lv, as + p * 2048 + (i & 1) * 147456); } };
    sfor<K4_X0>(ldr);
    { const char* sp = ws + WS_SSQ + tt * 512; sfor<8>([&](auto I) { gl4c<I * 64>(sq[I], (lane & 15) * 4, sp); }); }
    float A1[2], A3[2], IR2[2];
    unsigned adst = WS_A + ((tt * 2 + (lane >> 5)) * 144) * 1024 + (lane & 31) * 16;
    char* abase = ws + ((fs >> 3) * 36 + ((fs >> 1) & 3)) * 1024 + (fs & 1) * 512;
    unsigned la = lv, lb = lv + 49152, lc = lv + 98304; PIN(la); PIN(lb); PIN(lc);
    unsigned ld, le, lf; u32x2 fv;
    f32x4 acc4[3][4], acc[9][2]; u32x4 wf[FPF]; unsigned pk[4]; float ez[2], ew[2];
    auto rdw = [&](auto G) { constexpr int g = G;
        if constexpr (g < 324) { constexpr int s = g % 144; wf[g % FPF] = lds128((s < 48 ? la : s < 96 ? lb : lc) + (s % 48) * 1024); }
        else { constexpr int q = g - 324, s = (36 + 18 * (q / 9) + q % 9) % 144; wf[g % FPF] = lds128((s < 54 ? ld : s < 108 ? le : lf) + (s % 54) * 1024); } };
    auto epi = [&](auto E, auto I) { constexpr int e = E, i = I, pr = i / 17, st = i % 17, b = pr >> 1, h = st & 1, c = (pr & 1) * 2 + h; f32x4 (&a)[4] = acc4[e % 3];
        if constexpr (i < 68) {
            if constexpr (st < 2) ez[h] = a[b][c] * a[b][c];
            else if constexpr (st < 4) ez[h] = fmaf(ez[h], A3[b], A1[b]);
            else if constexpr (st < 6) ez[h] *= a[b][c];
            else if constexpr (st < 8) ez[h] = __builtin_amdgcn_exp2f(ez[h]);
            else if constexpr (st < 10) ew[h] = a[b][c] * a[2 + b][c];
            else if constexpr (st < 12) ez[h] = fmaf(ez[h], IR2[b], IR2[b]);
            else if constexpr (st < 14) ez[h] = __builtin_amdgcn_rcpf(ez[h]);
            else if constexpr (st < 16) ew[h] *= ez[h];
            else pk[pr] = cvtpk(ew[0], ew[1]);
            if constexpr (st == 16) PIN(pk[pr]); else if constexpr (st == 8 || st == 9 || st >= 14) PIN(ew[h]); else PIN(ez[h]);
        } else if constexpr (i == 68) { const auto r0 = __builtin_amdgcn_permlane32_swap(pk[0], pk[2], false, false); pk[0] = r0[0]; pk[2] = r0[1]; PIN(pk[0]); PIN(pk[2]); }
        else { const auto r1 = __builtin_amdgcn_permlane32_swap(pk[1], pk[3], false, false); gs16c(adst, abase + e * 4096, u32x4{pk[0], r1[0], pk[2], r1[1]}); } };
    auto gap = [&](auto G) { sfor<9>([&](auto E) { constexpr int f = k4_epi_first(E, G), l = k4_epi_first(E, G + 1); sfor<l - f>([&](auto I) { epi(E, IC<f + I>{}); }); }); SBAR(); };
    sfor<FPF - 1>([&](auto I) { rdw(I); });
    sfor<NSLOT>([&](auto G) { constexpr int g = G;
        constexpr int stg = g == 312 || g == 300 ? 0 : g == 378 || g == 366 ? 1 : g == 414 || g == 402 ? 2 : -1;
        if constexpr (f_bar(g)) {
            if constexpr (stg >= 0) { if (w == 1) { asm volatile("s_waitcnt vmcnt(%1)" : "+v"(fv) : "i"(f_after(g - 13, g)) : "memory");
                if (!__all(fv[0] == tlo && fv[1] == thi)) poll_flags(ws, WS_F4 + (qb * 32 + s4 * 8 + (lane & 7)) * 128 + stg * 8, true, tlo, thi); } }
            static_assert(f_barwait(g) >= 0, "needed round not issued");
            waitv_bar<f_barwait(g)>();
            if constexpr (g == 264 || g == 324 || g == 360) { if (w == 0 && lane == 0) set_flag(ws, WS_F4 + (qb * 32 + fs) * 128 + (g == 264 ? 0 : g == 324 ? 8 : 16), tlo, thi); }
        } else if constexpr (stg >= 0) { if (w == 1) gl8c<stg * 8>(fv, WS_F4 + (qb * 32 + s4 * 8 + (lane & 7)) * 128, ws); }
        if constexpr (FS.wn[g + 1] > FS.wn[g]) wround<FS.wn[g]>(S, w);
        if constexpr (FS.ln[g + 1] > FS.ln[g]) ldr(IC<FS.ln[g]>{});
        if constexpr (g == 108) KT_MARK(14); if constexpr (g == 216) KT_MARK(15); if constexpr (g == 325) { KT_MARK(16); KT_MARK(17); KT_MARK(18); } if constexpr (g == 405) KT_MARK(19);
        if constexpr (g == 316) { ld = lv + kg * 9216; le = ld + 54 * 1024; lf = ld + 108 * 1024; PIN(ld); PIN(le); PIN(lf); }
        if constexpr (g < 324) { constexpr int t = k4_tile(g), ks = k4_ks(g), x0 = ks & ~1, m = ks & 1;
            if constexpr (t == 0 && g < 84 && m == 0) { static_assert(FS.ln[g + 1] >= ks + 2, "X not issued");
                constexpr int n = (FS.ln[g + 1] - ks - 2) + (FS.wn[g + 1] - FS.wn[FS.lslot[ks + 1] + 1]) + (ks + 1 < K4_X0 ? 8 : 0);
                asm volatile("s_waitcnt vmcnt(%2)" : "+v"(xf[ks]), "+v"(xf[ks + 1]) : "i"(n) : "memory"); }
            if constexpr (ks < 2) mfma16z(acc4[t % 3][2 * m], wf[g % FPF], xf[x0]); else mfma16a(acc4[t % 3][2 * m], wf[g % FPF], xf[x0]);
            SBAR();
            if constexpr (g == 40) { asm volatile("" : "+v"(sq[0]), "+v"(sq[1]), "+v"(sq[2]), "+v"(sq[3]), "+v"(sq[4]), "+v"(sq[5]), "+v"(sq[6]), "+v"(sq[7]));
                UNR for (int b = 0; b < 2; ++b) {
                    const float var = ((__uint_as_float(sq[b]) + __uint_as_float(sq[2 + b])) + (__uint_as_float(sq[4 + b]) + __uint_as_float(sq[6 + b]))) * (1.f / 576.f) + 1e-5f;
                    const float rs = __builtin_amdgcn_rsqf(var);
                    A1[b] = -2.f * 0.7978845608028654f * 1.4426950408889634f * rs; A3[b] = A1[b] * 0.044715f * rs * rs; IR2[b] = var; PIN(A1[b]); PIN(A3[b]); PIN(IR2[b]); } }
            gap(IC<2 * g>{}); rdw(IC<g + FPF - 1>{}); SBAR();
            if constexpr (ks < 2) mfma16z(acc4[t % 3][2 * m + 1], wf[g % FPF], xf[x0 + 1]); else mfma16a(acc4[t % 3][2 * m + 1], wf[g % FPF], xf[x0 + 1]);
            SBAR();
            gap(IC<2 * g + 1>{});
        } else { constexpr int q = g - 324, p = q / 9, T = q % 9;
            if constexpr (T == 0) { constexpr int i = 36 + 2 * p + 1; static_assert(FS.ln[g + 1] > i, "a not issued");
                constexpr int n = (FS.ln[g + 1] - i - 1) + (FS.wn[g + 1] - FS.wn[FS.lslot[i] + 1]);
                asm volatile("s_waitcnt vmcnt(%2)" : "+v"(af[p % K5_AW][0]), "+v"(af[p % K5_AW][1]) : "i"(n) : "memory"); }
            if constexpr (p == 0) mfma16z(acc[T][0], wf[g % FPF], af[0][0]); else mfma16a(acc[T][0], wf[g % FPF], af[p % K5_AW][0]);
            SBAR();
            gap(IC<2 * g>{}); if constexpr (g + FPF - 1 < NSLOT) rdw(IC<g + FPF - 1>{}); SBAR();
            if constexpr (p == 0) mfma16z(acc[T][1], wf[g % FPF], af[0][1]); else mfma16a(acc[T][1], wf[g % FPF], af[p % K5_AW][1]);
            SBAR();
            gap(IC<2 * g + 1>{}); }
    });
    asm volatile("s_nop 7" : "+v"(acc[7][0]), "+v"(acc[7][1]), "+v"(acc[8][0]), "+v"(acc[8][1]));
    KT_MARK(20);
    const int q = lane >> 4, gt = m128 * 128 + tq * 32 + kg * 16 + (lane & 15), grp = m128 * 4 + j;
    u32x4 hr[4]; u32x2 h9; unsigned cmu;
    if (tq == s4) { const char* hs = ws + WS_HC + (m128 * 8 + tq * 2 + kg) * 18432 + j * 4608;
        gl16c<0>(hr[0], lv, hs); gl16c<1024>(hr[1], lv, hs); gl16c<2048>(hr[2], lv, hs); gl16c<3072>(hr[3], lv, hs);
        gl8c<0>(h9, (q >> 1) * 256 + (lane & 15) * 16 + (q & 1) * 8, hs + 4096); gl4c<0>(cmu, gt * 4, ws + WS_CM); }
    UNR for (int t = 0; t < 9; ++t) *(LAS f32x4*)(73728 + w * 9216 + t * 1024 + lv) = kg ? acc[t][0] : acc[t][1];
    lbar();
    f32x4 v[9];
    UNR for (int t = 0; t < 9; ++t) v[t] = (kg ? acc[t][1] : acc[t][0]) + *(const LAS f32x4*)(73728 + (w ^ 4) * 9216 + t * 1024 + lv);
    KT_MARK(21);
    if (tq != s4) { char* dst = ws + WS_XB + (grp * 4 + tq) * 32768 + (((s4 - tq - 1) & 3) * 2 + kg) * 4608;
        UNR for (int e = 0; e < 4; ++e) gs16c(lv + e * 1024, dst, u32x4{cvtpk(v[0][e], v[1][e]), cvtpk(v[2][e], v[3][e]), cvtpk(v[4][e], v[5][e]), cvtpk(v[6][e], v[7][e])});
        gs8c(4096 + lane * 8, dst, u32x2{cvtpk(v[8][0], v[8][1]), cvtpk(v[8][2], v[8][3])});
        if (lane == 0) gs4c(WS_XB + (grp * 4 + tq) * 32768 + 27648 + (((s4 - tq - 1) & 3) * 2 + kg) * 128, ws, 0u);
    } else {
        asm volatile("s_waitcnt vmcnt(0)" : "+v"(hr[0]), "+v"(hr[1]), "+v"(hr[2]), "+v"(hr[3]), "+v"(h9), "+v"(cmu) :: "memory");
        const float cm = __uint_as_float(cmu);
        UNR for (int e = 0; e < 4; ++e) { UNR for (int i = 0; i < 4; ++i) { v[2 * i][e] += bflo(hr[e][i]) + cm; v[2 * i + 1][e] += bfhi(hr[e][i]) + cm; } }
        v[8][0] += bflo(h9[0]) + cm; v[8][1] += bfhi(h9[0]) + cm; v[8][2] += bflo(h9[1]) + cm; v[8][3] += bfhi(h9[1]) + cm;
        UNR for (int t = 0; t < 9; ++t) PIN(v[t]);
        for (unsigned spins = 0; spins < (1u << 20); ++spins) { unsigned hv; gl4c<0>(hv, WS_XB + (grp * 4 + s4) * 32768 + 27648 + ((lane < 3 ? lane : 0) * 2 + kg) * 128, ws);
            asm volatile("s_waitcnt vmcnt(0)" : "+v"(hv) :: "memory"); if (__all(hv == 0u)) break; }
        u32x4 pa[3][4]; u32x2 pb[3]; const char* src = ws + WS_XB + (grp * 4 + s4) * 32768 + kg * 4608;
        for (unsigned spins = 0; spins < (1u << 16); ++spins) {
            sfor<3>([&](auto D) { constexpr int d = D;
                gl16c<0>(pa[d][0], lv, src + d * 9216); gl16c<1024>(pa[d][1], lv, src + d * 9216); gl16c<2048>(pa[d][2], lv, src + d * 9216); gl16c<3072>(pa[d][3], lv, src + d * 9216); gl8c<0>(pb[d], lane * 8, src + d * 9216 + 4096); });
            asm volatile("s_waitcnt vmcnt(0)" : "+v"(pa[0][0]), "+v"(pa[0][1]), "+v"(pa[0][2]), "+v"(pa[0][3]), "+v"(pa[1][0]), "+v"(pa[1][1]), "+v"(pa[1][2]), "+v"(pa[1][3]),
                         "+v"(pa[2][0]), "+v"(pa[2][1]), "+v"(pa[2][2]), "+v"(pa[2][3]), "+v"(pb[0]), "+v"(pb[1]), "+v"(pb[2]) :: "memory");
            unsigned mx = 0;
            UNR for (int d = 0; d < 3; ++d) { UNR for (int e = 0; e < 4; ++e) UNR for (int i = 0; i < 4; ++i) mx = mx > pa[d][e][i] ? mx : pa[d][e][i];
                mx = mx > pb[d][0] ? mx : pb[d][0]; mx = mx > pb[d][1] ? mx : pb[d][1]; }
            if (__all(mx != ~0u)) break; }
        UNR for (int d = 0; d < 3; ++d) { UNR for (int e = 0; e < 4; ++e) UNR for (int i = 0; i < 4; ++i) { v[2 * i][e] += bflo(pa[d][e][i]); v[2 * i + 1][e] += bfhi(pa[d][e][i]); }
            v[8][0] += bflo(pb[d][0]); v[8][1] += bfhi(pb[d][0]); v[8][2] += bflo(pb[d][1]); v[8][3] += bfhi(pb[d][1]); }
        char* od = out + (size_t)gt * 1152 + j * 288;
        UNR for (int e = 0; e < 4; ++e) *(u32x4*)(od + e * 64 + q * 16) = u32x4{cvtpk(v[0][e], v[1][e]), cvtpk(v[2][e], v[3][e]), cvtpk(v[4][e], v[5][e]), cvtpk(v[6][e], v[7][e])};
        *(u32x2*)(od + 256 + q * 8) = u32x2{cvtpk(v[8][0], v[8][1]), cvtpk(v[8][2], v[8][3])};
    }
}

#define KT_FWD

__global__ __launch_bounds__(512, 2) void block_fwd(const char* __restrict__ x, const char* __restrict__ wqkv, const char* __restrict__ wo, const char* __restrict__ wg,
                                                    const char* __restrict__ wu, const char* __restrict__ wd, char* __restrict__ ws, char* __restrict__ out KT_ARG) {
    extern __shared__ __attribute__((aligned(16))) char smem[];
    const int w = rfl(threadIdx.x >> 6), xc = blockIdx.x & 7, rk = blockIdx.x >> 3, hs = rk & 3, qb = rk >> 2;
    KT_MARK(0);
    u32x2 ep; gl8c<0>(ep, WS_EPOCH, ws);
    const int mt1 = (xc >> 1) * 8 + (rk & 7), ct1 = (xc & 1) * 4 + (rk >> 3);
    phase1(x, wqkv, wo, ws, w, mt1, ct1 KT_FWD);
    asm volatile("s_waitcnt vmcnt(0) lgkmcnt(0)\n\ts_barrier" : "+v"(ep) :: "memory");
    KT_MARK(4);
    const u64 tag = (((u64)rfl(ep[1]) << 32) | rfl(ep[0])) + 1; const unsigned tlo = (unsigned)tag, thi = (unsigned)(tag >> 32);
    RtTask RT;
    if (w == 0) { const int lane = lane_id(), pp = xc * 2 + (hs >> 1);
        if (lane < 2) set_flag(ws, lane ? WS_FD + blockIdx.x * 8 : WS_F1 + (ct1 * 32 + mt1) * 128, tlo, thi);
        rt_setup(RT, wg, wu, wd, blockIdx.x * 8);
        poll_flags2(ws, WS_F1 + (((lane < 32 ? 16 : 32) + pp) / 6 * 32 + (lane & 31)) * 128, WS_F1 + (pp / 6 * 32 + qb * 4 + (lane & 3)) * 128, tlo, thi); }
    else rt_setup(RT, wg, wu, wd, blockIdx.x * 8 + w);
    asm volatile("s_barrier" ::: "memory");
    KT_MARK(5);
    P3Pre P;
    const int mt4 = qb, fs4 = xc * 4 + hs, m5 = qb * 2 + (xc & 1), j5 = hs, s5 = xc >> 1;
    phase2(x, RT, ws, P, w, xc * 4 + hs, qb, qb * 8 + xc, hs, (m5 * 4 + j5) * 4 + s5, tlo, thi KT_FWD);
    if (w == 0) poll_dense(ws, WS_FR, lane_id(), tlo, thi);
    KT_MARK(8);
    fullbar();
    UNR for (int i = 0; i < 5; ++i) asm volatile("" : "+v"(P.wf[i][0]), "+v"(P.wf[i][1]), "+v"(P.wf[i][2]), "+v"(P.wf[i][3]), "+v"(P.wf[i][4]));
    asm volatile("" : "+v"(P.xa), "+v"(P.xb), "+v"(P.xm));
    KT_MARK(9);
    if (w == 0 && lane_id() == 0) set_flag(ws, WS_F2 + (qb * 32 + xc * 4 + hs) * 128, tlo, thi);
    WStream S; S.lv = lane_id() * 16; S.lw = w * 1024;
    S.a = ws + WS_WGU + fs4 * 324 * 1024 + w * 1024;
    S.b = ws + WS_WD + (j5 * 144 + s5 * 36) * 9216 + w * 1024 - 4096;
    sfor<9>([&](auto J) { wround<J>(S, w); });
    if (w == 0) poll_flags(ws, WS_F2 + (qb * 32 + (lane_id() & 31)) * 128, true, tlo, thi);
    asm volatile("s_barrier" ::: "memory");
    KT_MARK(10);
    phase3(ws, P, w, qb * 8 + xc, hs KT_FWD);
    fullbar();
    KT_MARK(12);
    if (w == 0 && lane_id() == 0) set_flag(ws, WS_F3 + (qb * 32 + xc * 4 + hs) * 128, tlo, thi);
    sfor<9>([&](auto J) { wround<9 + J>(S, w); });
    if (w == 0) poll_flags(ws, WS_F3 + (mt4 * 32 + (lane_id() & 31)) * 128, true, tlo, thi);
    asm volatile("s_barrier" ::: "memory");
    KT_MARK(13);
    phase45(ws, out, S, w, qb, fs4, m5, j5, s5, tlo, thi KT_FWD);
    if (blockIdx.x == 0 && w == 0 && lane_id() == 0) set_flag(ws, WS_EPOCH, tlo, thi);
    KT_MARK(22);
}

#define KT_PASS

extern "C" void kernel_launch(void* const* d_in, const int* in_sizes, int n_in, void* d_out, int out_size, void* d_ws, size_t ws_size, hipStream_t stream) {
    (void)hipFuncSetAttribute((const void*)block_fwd, hipFuncAttributeMaxDynamicSharedMemorySize, LDS_BYTES);
    block_fwd<<<256, 512, LDS_BYTES, stream>>>((const char*)d_in[0], (const char*)d_in[1], (const char*)d_in[2], (const char*)d_in[4], (const char*)d_in[5], (const char*)d_in[6],
                                                (char*)d_ws, (char*)d_out KT_PASS);
}
```

```cpp
#include <hip/hip_runtime.h>
#include <hip/hip_bf16.h>
#include <stdint.h>
#include <utility>

typedef float f32x16 __attribute__((ext_vector_type(16)));
typedef float f32x4 __attribute__((ext_vector_type(4)));
typedef float f32x2 __attribute__((ext_vector_type(2)));
typedef __bf16 bf16x8 __attribute__((ext_vector_type(8)));
typedef __bf16 bf16x2 __attribute__((ext_vector_type(2)));
typedef short s16x4 __attribute__((ext_vector_type(4)));
typedef unsigned u32x4 __attribute__((ext_vector_type(4)));
typedef unsigned u32x2 __attribute__((ext_vector_type(2)));
typedef unsigned long long u64;
#define LAS __attribute__((address_space(3)))
#define GAS __attribute__((address_space(1)))
#define DI __device__ __forceinline__
#define HD __host__ __device__
#define SBAR() __builtin_amdgcn_sched_barrier(0)
#define PIN(x) asm volatile("" : "+v"(x))
#define UNR _Pragma("unroll")
template <int V> using IC = std::integral_constant<int, V>;

#define ROT1(mt) ((mt) & 7)
constexpr unsigned QIMG = 2304, KVIMG = 4864;
constexpr unsigned WS_EPOCH = 0;
constexpr unsigned WS_FR = 256, WS_FD = WS_FR + 2048;
constexpr unsigned WS_F1 = 8192, WS_F2 = WS_F1 + 32768, WS_F3 = WS_F2 + 32768, WS_F4 = WS_F3 + 32768;
constexpr unsigned WS_XMEAN = WS_F4 + 32768;
constexpr unsigned WS_CM = WS_XMEAN + 8192;
constexpr unsigned WS_SSQ = WS_CM + 8192;
constexpr unsigned WS_WBAR = WS_SSQ + 32768;
constexpr unsigned WS_Q = WS_WBAR + 18432;
constexpr unsigned WS_KV = WS_Q + 32 * 32 * QIMG;
constexpr unsigned WS_O = WS_KV + 32 * 32 * KVIMG;
constexpr unsigned WS_OR = WS_O + 128 * 16 * 1024;
constexpr unsigned WS_HC = WS_OR + 64 * 32 * 128;
constexpr unsigned WS_WO = WS_HC + 64 * 36 * 1024;
constexpr unsigned WS_WGU = WS_WO + 4 * 18 * 9 * 1024;
constexpr unsigned WS_WD = WS_WGU + 32 * 324 * 1024;
constexpr unsigned WS_A = WS_WD + 4 * 144 * 9 * 1024;
constexpr unsigned WS_XB = WS_A + 128 * 144 * 1024;
constexpr unsigned WS_END = WS_XB + 256 * 32768;
constexpr int LDS_BYTES = 163840;

DI unsigned rfl(unsigned x) { return __builtin_amdgcn_readfirstlane(x); }
DI unsigned cvtpk(float lo, float hi) { f32x2 v = {lo, hi}; bf16x2 b = __builtin_convertvector(v, bf16x2); return __builtin_bit_cast(unsigned, b); }
DI float bflo(unsigned u) { return __uint_as_float(u << 16); }
DI float bfhi(unsigned u) { return __uint_as_float(u & 0xffff0000u); }
DI float dot2(unsigned a, unsigned b, float c) { return __builtin_amdgcn_fdot2_f32_bf16(__builtin_bit_cast(bf16x2, a), __builtin_bit_cast(bf16x2, b), c, false); }
DI u32x4 addc8(const u32x4& v, unsigned lo, unsigned hi, float c) { float t0, t1, t2, t3, t4, t5, t6, t7;
    asm("v_dot2_f32_bf16 %0, %8, %12, %14\n\tv_dot2_f32_bf16 %1, %8, %13, %14\n\tv_dot2_f32_bf16 %2, %9, %12, %14\n\tv_dot2_f32_bf16 %3, %9, %13, %14\n\t"
        "v_dot2_f32_bf16 %4, %10, %12, %14\n\tv_dot2_f32_bf16 %5, %10, %13, %14\n\tv_dot2_f32_bf16 %6, %11, %12, %14\n\tv_dot2_f32_bf16 %7, %11, %13, %14\n\ts_nop 2"
        : "=&v"(t0), "=&v"(t1), "=&v"(t2), "=&v"(t3), "=&v"(t4), "=&v"(t5), "=&v"(t6), "=&v"(t7) : "v"(v[0]), "v"(v[1]), "v"(v[2]), "v"(v[3]), "v"(lo), "v"(hi), "v"(c));
    return u32x4{cvtpk(t0, t1), cvtpk(t2, t3), cvtpk(t4, t5), cvtpk(t6, t7)}; }
DI f32x16 mfma32(u32x4 a, u32x4 b, f32x16 c) { return __builtin_amdgcn_mfma_f32_32x32x16_bf16(__builtin_bit_cast(bf16x8, a), __builtin_bit_cast(bf16x8, b), c, 0, 0, 0); }
DI f32x16 mfma32k8(u32x2 a, u32x2 b, f32x16 c) { return __builtin_amdgcn_mfma_f32_32x32x8bf16_1k(__builtin_bit_cast(s16x4, a), __builtin_bit_cast(s16x4, b), c, 0, 0, 0); }
DI f32x4 mfma16(u32x4 a, u32x4 b, f32x4 c) { return __builtin_amdgcn_mfma_f32_16x16x32_bf16(__builtin_bit_cast(bf16x8, a), __builtin_bit_cast(bf16x8, b), c, 0, 0, 0); }
DI void mfma16a(f32x4& c, const u32x4& a, const u32x4& b) { asm volatile("v_mfma_f32_16x16x32_bf16 %0, %1, %2, %0" : "+v"(c) : "v"(a), "v"(b)); }
DI void mfma16z(f32x4& c, const u32x4& a, const u32x4& b) { asm volatile("v_mfma_f32_16x16x32_bf16 %0, %1, %2, 0" : "=&v"(c) : "v"(a), "v"(b)); }
DI void glds16(unsigned voff, const void* sb, unsigned lds) {
    asm volatile("s_mov_b32 m0, %2\n\ts_nop 0\n\tglobal_load_lds_dwordx4 %0, %1" :: "v"(voff), "s"(sb), "s"(lds) : "memory"); }
DI void glds16c(unsigned voff, const void* sb, unsigned lds) {
    asm volatile("s_mov_b32 m0, %2\n\ts_nop 0\n\tglobal_load_lds_dwordx4 %0, %1 sc1" :: "v"(voff), "s"(sb), "s"(lds) : "memory"); }
DI void touch(unsigned voff, const void* sb, unsigned lds) {
    asm volatile("s_mov_b32 m0, %2\n\ts_nop 0\n\tglobal_load_lds_dword %0, %1" :: "v"(voff), "s"(sb), "s"(lds) : "memory"); }
template <int OFF> DI void gl16(u32x4& d, unsigned voff, const void* sb) { asm volatile("global_load_dwordx4 %0, %1, %2 offset:%3" : "=v"(d) : "v"(voff), "s"(sb), "i"(OFF) : "memory"); }
template <int OFF> DI void gl16c(u32x4& d, unsigned voff, const void* sb) { asm volatile("global_load_dwordx4 %0, %1, %2 offset:%3 sc1" : "=v"(d) : "v"(voff), "s"(sb), "i"(OFF) : "memory"); }
template <int OFF> DI void gl8c(u32x2& d, unsigned voff, const void* sb) { asm volatile("global_load_dwordx2 %0, %1, %2 offset:%3 sc1" : "=v"(d) : "v"(voff), "s"(sb), "i"(OFF) : "memory"); }
template <int OFF> DI void gl4c(unsigned& d, unsigned voff, const void* sb) { asm volatile("global_load_dword %0, %1, %2 offset:%3 sc1" : "=v"(d) : "v"(voff), "s"(sb), "i"(OFF) : "memory"); }
DI void gs16c(unsigned voff, void* sb, u32x4 v) { asm volatile("global_store_dwordx4 %0, %1, %2 sc1\n\ts_nop 1" :: "v"(voff), "v"(v), "s"(sb) : "memory"); }
DI void gs8c(unsigned voff, void* sb, u32x2 v) { asm volatile("global_store_dwordx2 %0, %1, %2 sc1" :: "v"(voff), "v"(v), "s"(sb) : "memory"); }
DI void gs4c(unsigned voff, void* sb, unsigned v) { asm volatile("global_store_dword %0, %1, %2 sc1" :: "v"(voff), "v"(v), "s"(sb) : "memory"); }
template <int N> DI void waitv() { asm volatile("s_waitcnt vmcnt(%0)" :: "i"(N) : "memory"); }
template <int N> DI void waitv_bar() { asm volatile("s_waitcnt vmcnt(%0)\n\ts_barrier" :: "i"(N) : "memory"); }
template <int N> DI void waitv_pin(u32x4& a) { asm volatile("s_waitcnt vmcnt(%1)" : "+v"(a) : "i"(N) : "memory"); }
DI void lbar() { asm volatile("s_waitcnt lgkmcnt(0)\n\ts_barrier" ::: "memory"); }
DI void fullbar() { asm volatile("s_waitcnt vmcnt(0) lgkmcnt(0)\n\ts_barrier" ::: "memory"); }
DI u32x4 lds128(unsigned a) { return *(const LAS u32x4*)(a); }
DI u32x2 lds64(unsigned a) { return *(const LAS u32x2*)(a); }
DI void sts128(unsigned a, u32x4 v) { *(LAS u32x4*)(a) = v; }
DI u32x2 ldstr(unsigned a) { return __builtin_bit_cast(u32x2, __builtin_amdgcn_ds_read_tr16_b64_v4i16((LAS s16x4*)(a))); }
template <int CTRL> DI float dpp_add(float v) { return v + __uint_as_float(__builtin_amdgcn_update_dpp(0u, __float_as_uint(v), CTRL, 0xf, 0xf, true)); }
DI float red8(float v) { v = dpp_add<0xB1>(v); v = dpp_add<0x4E>(v); return dpp_add<0x141>(v); }
DI float red16(float v) { v = red8(v); return dpp_add<0x140>(v); }
template <class F, int... Is> DI void sfor_impl(F&& f, std::integer_sequence<int, Is...>) { (f(IC<Is>{}), ...); }
template <int N, class F> DI void sfor(F&& f) { sfor_impl(f, std::make_integer_sequence<int, N>{}); }
DI int lane_id() { int l; asm volatile("v_mbcnt_lo_u32_b32 %0, -1, 0\n\tv_mbcnt_hi_u32_b32 %0, -1, %0" : "=v"(l)); return l; }

DI void set_flag(char* ws, unsigned off, unsigned tlo, unsigned thi) { gs8c(off, ws, u32x2{tlo, thi}); }
DI void poll_flags(const char* ws, unsigned off, bool need, unsigned tlo, unsigned thi) {
    for (unsigned spins = 0; spins < (1u << 20); ++spins) { u32x2 v; gl8c<0>(v, off, ws); asm volatile("s_waitcnt vmcnt(0)" : "+v"(v) :: "memory");
        if (__all(!need || (v[0] == tlo && v[1] == thi))) break; } }
DI void poll_flags2(const char* ws, unsigned o1, unsigned o2, unsigned tlo, unsigned thi) {
    for (unsigned spins = 0; spins < (1u << 20); ++spins) { u32x2 v, u; gl8c<0>(v, o1, ws); gl8c<0>(u, o2, ws); asm volatile("s_waitcnt vmcnt(0)" : "+v"(v), "+v"(u) :: "memory");
        if (__all(v[0] == tlo && v[1] == thi && u[0] == tlo && u[1] == thi)) break; } }
DI void poll_dense(const char* ws, unsigned base, int lane, unsigned tlo, unsigned thi) {
    for (unsigned spins = 0; spins < (1u << 20); ++spins) { u32x4 v, u; gl16c<0>(v, base + lane * 16, ws); gl16c<1024>(u, base + lane * 16, ws); asm volatile("s_waitcnt vmcnt(0)" : "+v"(v), "+v"(u) :: "memory");
        if (__all(v[0] == tlo && v[1] == thi && v[2] == tlo && v[3] == thi && u[0] == tlo && u[1] == thi && u[2] == tlo && u[3] == thi)) break; __builtin_amdgcn_s_sleep(4); } }

HD constexpr int vslot(int r) { return r < 4 ? r : r < 12 ? r + 6 : r < 16 ? r - 6 : r + 2; }
HD constexpr int k4pos(int t, int ks) { return t >= 3 ? 36 * t + ks : (ks < 24 ? 3 * ks + t : 72 + 12 * t + (ks - 24)); }

#define KT_ARG
#define KT_MARK(i)

struct Retile { u32x4 in[8]; unsigned e[8]; };
template <int J> DI void rt_load(Retile& R, const char* p, int lo, int hi) { const char* a = p + ((J & 3) * lo + (J >> 2) * hi);
    asm volatile("global_load_dwordx4 %0, %1, off" : "=v"(R.in[J]) : "v"(a) : "memory"); }
template <int J> DI void rt_load2(Retile& R, const char* p, int lo, int hi) { const char* a = p + ((J & 3) * lo + (J >> 2) * hi);
    asm volatile("global_load_ushort %0, %1, off" : "=v"(R.e[J]) : "v"(a) : "memory"); }
DI void rt_wait(Retile& R) { asm volatile("s_waitcnt vmcnt(0)" : "+v"(R.in[0]), "+v"(R.in[1]), "+v"(R.in[2]), "+v"(R.in[3]), "+v"(R.in[4]), "+v"(R.in[5]), "+v"(R.in[6]), "+v"(R.in[7]) :: "memory"); }
template <int I> DI u32x4 rt_col(const Retile& R) { u32x4 o;
    UNR for (int k = 0; k < 4; ++k) o[k] = __builtin_amdgcn_perm(R.in[2 * k + 1][I >> 1], R.in[2 * k][I >> 1], (I & 1) ? 0x07060302u : 0x05040100u); return o; }
DI u32x4 rt_col9(Retile& R) { asm volatile("" : "+v"(R.e[0]), "+v"(R.e[1]), "+v"(R.e[2]), "+v"(R.e[3]), "+v"(R.e[4]), "+v"(R.e[5]), "+v"(R.e[6]), "+v"(R.e[7]));
    return u32x4{R.e[0] | R.e[1] << 16, R.e[2] | R.e[3] << 16, R.e[4] | R.e[5] << 16, R.e[6] | R.e[7] << 16}; }

constexpr int K1_XB = 528, K1_W = 2 * 72 * K1_XB, K1_BLK = 7168, K1_RS = K1_W + 12 * K1_BLK;

DI int wrap36(int v) { return v >= 36 ? v - 36 : v; }
template <int MODE> DI void p1_loop(f32x16& c0, f32x16& c1, unsigned xa, unsigned xb, unsigned wa, const unsigned (&wv)[7], const char* wq, int w, int rot) {
    u32x4 wf[8], x0[2], x1[2];
    auto rw = [&](auto K) { constexpr int ks = K; const u32x2 w0 = ldstr(wa + (ks % 12) * K1_BLK), w1 = ldstr(wa + (ks % 12) * K1_BLK + 1792); wf[ks % 8] = u32x4{w0[0], w0[1], w1[0], w1[1]}; };
    auto rx = [&](auto K) { constexpr int ks = K; x0[ks & 1] = lds128(xa + ks * 2 * K1_XB); x1[ks & 1] = lds128(xb + ks * 2 * K1_XB); };
    if constexpr (MODE != 2) { sfor<4>(rw); rx(IC<0>{}); }
    sfor<9>([&](auto P) { constexpr int p = P;
        if constexpr (p < 8) { if ((w >> 2) == ((p + 1) & 1)) waitv<0>(); lbar(); }
        const bool own = p <= 5 && (w >> 2) == ((p + 1) & 1);
        const int blk = 4 * (p + 3) + (w & 3); const char* s = wq + wrap36(blk + rot) * (16 * 3456); const unsigned l = K1_W + (blk % 12) * K1_BLK;
        if constexpr (MODE == 2) { if (own) { UNR for (int i = 0; i < 7; ++i) glds16(wv[i], s, l + i * 1024); } }
        else sfor<4>([&](auto KK) { constexpr int ks = 4 * p + KK;
            if constexpr (ks + 1 < 36) rx(IC<ks + 1>{});
            if (own) glds16(wv[2 * KK], s, l + 2 * KK * 1024);
            if constexpr (MODE) c0 = mfma32(x0[ks & 1], wf[ks % 8], c0); else c0 = mfma32(wf[ks % 8], x0[ks & 1], c0);
            SBAR();
            if constexpr (ks + 4 < 36) rw(IC<ks + 4>{});
            if constexpr (KK < 3) { if (own) glds16(wv[2 * KK + 1], s, l + (2 * KK + 1) * 1024); }
            if constexpr (MODE) c1 = mfma32(x1[ks & 1], wf[ks % 8], c1); else c1 = mfma32(wf[ks % 8], x1[ks & 1], c1);
            SBAR(); });
    });
}

DI void phase1(const char* x, const char* wqkv, const char* wo, char* ws, int w, int mt, int ct KT_ARG) {
    const int lane = lane_id(), tid = w * 64 + lane;
    u32x4 xv[9];
    { const unsigned vo = (unsigned)((w * 8 + (lane >> 3)) * 1152 + (lane & 7) * 16); const char* xs = x + (size_t)mt * 64 * 1152;
      sfor<9>([&](auto I) { const int g9 = I + ROT1(mt); gl16<0>(xv[I], vo, xs + (g9 >= 9 ? g9 - 9 : g9) * 128); }); }
    const bool idle = (w == 7 && ct < 5) || (w == 6 && ct > 5); const int id = (ct < 5 ? ct : ct - 1) * 32 + mt; Retile R; u32x4 r9;
    if (idle && id < 144) { const bool fr = id < 72; const int j = id & 3, u = (fr ? id : id - 72) >> 2, rr = lane & 15, gq = fr ? lane >> 4 : j, oc = 4 * (rr & 3) + (rr >> 2), jr = fr ? 0 : lane >> 3;
        const int row = u < 16 ? 36 * u + (gq >> 1) * 20 + 4 * (gq & 1) + (jr & 3) + (jr >> 2) * 8 : 36 * (8 * (u - 16) + 2 * gq + (jr >> 2)) + 16 + (jr & 3);
        const char* p = wo + (row * 1152 + (fr ? j * 288 + oc * 16 : (lane & 7) * 16)); const int lo = fr ? 1152 : 128, hi = fr ? (u < 16 ? 9216 : 41472) : 512;
        sfor<8>([&](auto J) { rt_load<J>(R, p, lo, hi); rt_load2<J>(R, p + (fr ? 256 + rr * 2 - oc * 16 : 0), lo, hi); });
        asm volatile("global_load_dwordx4 %0, %1, off" : "=v"(r9) : "v"(p + (fr ? 0 : 1024)) : "memory"); }
    unsigned wv[7];
    UNR for (int i = 0; i < 7; ++i) { const int c = i * 64 + lane, row = (c * 2341) >> 16, ch = c - row * 28; wv[i] = (unsigned)(row * 3456 + (ch < 27 ? ch : 26) * 16); }
    const int rot = ROT1(mt) * 4; const char* wq = wqkv + ct * 432; const char* wq0 = wq + wrap36(w + rot) * (16 * 3456); const char* wq1 = wq + wrap36(w + 8 + rot) * (16 * 3456);
    asm volatile("s_barrier" ::: "memory");
    auto dm = [&](auto I) { constexpr int i = I; SBAR();
        if constexpr (i < 7) glds16(wv[i], wq0, K1_W + w * K1_BLK + i * 1024);
        else { if (w < 4) glds16(wv[i - 7], wq1, K1_W + (w + 8) * K1_BLK + (i - 7) * 1024); } SBAR(); };
    auto dm1 = [&](auto I) { if (w >= 4) dm(I); };
    if (w < 4) { sfor<7>(dm); waitv<7>(); } else { sfor<2>(dm); waitv<2>(); }
    asm volatile("" : "+v"(xv[0]), "+v"(xv[1]), "+v"(xv[2]), "+v"(xv[3]), "+v"(xv[4]), "+v"(xv[5]), "+v"(xv[6]), "+v"(xv[7]), "+v"(xv[8]));
    KT_MARK(1);
    float mean; const int xrow = w * 8 + (lane >> 3);
    { float s0 = 0, s1 = 0, s2 = 0, s3 = 0; unsigned onev = 0x3f803f80u; PIN(onev);
      sfor<9>([&](auto I) { constexpr int i = I; s0 = dot2(xv[i][0], onev, s0); s1 = dot2(xv[i][1], onev, s1); s2 = dot2(xv[i][2], onev, s2); s3 = dot2(xv[i][3], onev, s3);
          if constexpr (i == 4) { PIN(s0); dm1(IC<2>{}); } });
      mean = red8((s0 + s1) + (s2 + s3)) * (1.f / 576.f); }
    { const unsigned xa = ((xrow >> 5) * 72 + (lane & 7)) * K1_XB + (xrow & 31) * 16;
      unsigned slo = 0x00003f80u, shi = 0x3f800000u; float nm = -mean; PIN(slo); PIN(shi); PIN(nm);
      sfor<9>([&](auto I) { constexpr int i = I; xv[i] = addc8(xv[i], slo, shi, nm); sts128(xa + i * 8 * K1_XB, xv[i]); if constexpr (i % 2 == 0 && i < 8) dm1(IC<3 + i / 2>{}); }); }
    if (w < 4) waitv<0>();
    lbar();
    KT_MARK(2);
    { float q0 = 0, q1 = 0, q2 = 0, q3 = 0;
      sfor<9>([&](auto I) { constexpr int i = I; q0 = dot2(xv[i][0], xv[i][0], q0); q1 = dot2(xv[i][1], xv[i][1], q1); q2 = dot2(xv[i][2], xv[i][2], q2); q3 = dot2(xv[i][3], xv[i][3], q3);
          if constexpr (i < 7) { PIN(q0); dm(IC<7 + i>{}); } });
      const float rstd = __builtin_amdgcn_rsqf(red8((q0 + q1) + (q2 + q3)) * (1.f / 576.f) + 1e-5f);
      if ((lane & 7) == 0) *(LAS float*)(K1_RS + xrow * 4) = rstd; }
    const int r = lane & 31, hh = lane >> 5;
    unsigned xa = hh * K1_XB + r * 16, xb = xa + 72 * K1_XB, wa; PIN(xa); PIN(xb);
    { const int cg = (lane >> 4) & 1, q = (lane & 15) >> 2, p = lane & 3;
      wa = K1_W + (8 * hh + q) * 448 + (w < 6 ? 72 * w + cg * 40 + 8 * p : 32 + 72 * (cg ? 4 + (p & 1) : p)); }
    f32x16 c0 = {}, c1 = {};
    const int gp = 6 * ct + w;
    const bool vor = w < 6 ? gp >= 32 : w == 7;
    if (idle) {
        p1_loop<2>(c0, c1, xa, xb, wa, wv, wq, w, rot);
        if (id < 144) { asm volatile("" : "+v"(R.in[0]), "+v"(R.in[1]), "+v"(R.in[2]), "+v"(R.in[3]), "+v"(R.in[4]), "+v"(R.in[5]), "+v"(R.in[6]), "+v"(R.in[7]), "+v"(r9));
            if (id < 72) { char* d = ws + WS_WO + ((id & 3) * 18 + (id >> 2)) * 9216;
                sfor<8>([&](auto I) { gs16c(lane * 16, d + I * 1024, rt_col<I>(R)); }); gs16c(lane * 16, d + 8192, rt_col9(R));
            } else { float s = 0.f; UNR for (int c = 0; c < 4; ++c) { s += bflo(r9[c]) + bfhi(r9[c]); UNR for (int i = 0; i < 8; ++i) s += bflo(R.in[i][c]) + bfhi(R.in[i][c]); }
                s = red8(s) * (1.f / 576.f);
                const unsigned hb = cvtpk(s, s) & 0xffffu; const float rest = s - bflo(hb); const unsigned both = hb | (cvtpk(rest, rest) & 0xffff0000u); u32x4 o;
                UNR for (int c = 0; c < 4; ++c) { const unsigned a = __builtin_amdgcn_readlane(both, 16 * c), b = __builtin_amdgcn_readlane(both, 16 * c + 8);
                    const unsigned v0 = (a & 0xffffu) | b << 16, v1 = (a >> 16) | (b & 0xffff0000u); o[c] = lane == 0 ? v0 : lane == 1 ? v1 : 0u; }
                if (lane < 16) gs16c(WS_WBAR + (id - 72) * 256 + lane * 16, ws, o); } }
    } else if (vor) p1_loop<1>(c0, c1, xa, xb, wa, wv, wq, w, rot); else p1_loop<0>(c0, c1, xa, xb, wa, wv, wq, w, rot);
    KT_MARK(3);
    constexpr float QS = 0.23570226039551584f * 1.4426950408889634f;
    constexpr unsigned st = K1_W, HB = 2560;
#define PK8(c, o, f) u32x4{cvtpk(c[o] * f, c[o + 1] * f), cvtpk(c[o + 2] * f, c[o + 3] * f), cvtpk(c[o + 4] * f, c[o + 5] * f), cvtpk(c[o + 6] * f, c[o + 7] * f)}
    if (!idle) {
        if (!vor) { const float r0 = *(const LAS float*)(K1_RS + r * 4), r1 = *(const LAS float*)(K1_RS + 128 + r * 4);
            if (w < 6) { const float f = gp < 16 ? QS : 1.f, f0 = f * r0, f1 = f * r1; const unsigned hb = st + 2 * w * HB + lane * 16;
                sts128(hb, PK8(c0, 0, f0)); sts128(hb + 1024, PK8(c1, 0, f1)); sts128(hb + HB, PK8(c0, 8, f0)); sts128(hb + HB + 1024, PK8(c1, 8, f1));
            } else { UNR for (int i = 0; i < 3; ++i) { const int pl = 2 * i + hh, g2 = 6 * ct + pl; const float f = g2 < 16 ? QS : 1.f, f0 = f * r0, f1 = f * r1;
                    UNR for (int e = 0; e < 2; ++e) { const unsigned hb = st + (2 * pl + e) * HB + 2048;
                        const unsigned v0 = cvtpk(c0[4 * i + 2 * e] * f0, c0[4 * i + 2 * e + 1] * f0), v1 = cvtpk(c1[4 * i + 2 * e] * f1, c1[4 * i + 2 * e + 1] * f1);
                        if (g2 < 32) { *(LAS unsigned*)(hb + (g2 < 16 ? r * 4 : r * 8)) = v0; *(LAS unsigned*)(hb + (g2 < 16 ? 128 + r * 4 : r * 8 + 4)) = v1; } } } }
        } else { const int hd = w < 6 ? 2 * w + (r >> 4) : r >> 1, rho = w < 6 ? r & 15 : 16 + (r & 1);
            UNR for (int i = 0; i < 4; ++i) { const f32x4 r0 = *(const LAS f32x4*)(K1_RS + i * 32 + hh * 16), r1 = *(const LAS f32x4*)(K1_RS + 128 + i * 32 + hh * 16);
                UNR for (int e = 0; e < 4; ++e) { c0[4 * i + e] *= r0[e]; c1[4 * i + e] *= r1[e]; } }
            if (w < 6 || (r < 24 && 6 * ct + (r >> 2) >= 32)) { const unsigned hb = st + hd * HB + hh * 320 + vslot(rho) * 16;
                sts128(hb, PK8(c0, 0, 1.f)); sts128(hb + 640, PK8(c0, 8, 1.f)); sts128(hb + 1280, PK8(c1, 0, 1.f)); sts128(hb + 1920, PK8(c1, 8, 1.f)); }
            if (w < 6 && r < 16) { const unsigned cv = hh ? 0u : 0x3f803f80u; sts128(st + 2 * w * HB + r * 320 + 64 + hh * 16, u32x4{cv, cv, cv, cv}); } } }
#undef PK8
    if (ct == 0 && (lane & 7) == 0) gs4c(WS_XMEAN + (mt * 64 + xrow) * 4, ws, __float_as_uint(mean));
    lbar();
    UNR for (int i = 0; i < 4; ++i) { const int u = tid + 512 * i, hd = (u * 6554) >> 20, o = (u - hd * 160) * 16, gh = 12 * ct + hd, sec = gh >> 5, hm = (gh & 31) * 32 + mt;
        if (u < 1920 && (sec == 2 || o < 2304)) gs16c(sec == 0 ? WS_Q + hm * QIMG + o : WS_KV + hm * KVIMG + (sec == 2 ? 2304 : 0) + o, ws, lds128(st + u * 16)); }
}

constexpr int K2_IMG = 32 * KVIMG, K2_HALF = 16 * KVIMG;
struct P3Pre { u32x4 wf[5][5], xa, xb; unsigned xm; };

DI float xhalf_max(float m) { auto rr = __builtin_amdgcn_permlane32_swap(__float_as_uint(m), __float_as_uint(m), false, false);
    return __builtin_fmaxf(__uint_as_float(rr[0]), __uint_as_float(rr[1])); }
DI float max16(const f32x16& s) { float a = __builtin_fmaxf(__builtin_fmaxf(s[0], s[1]), s[2]), b = __builtin_fmaxf(__builtin_fmaxf(s[3], s[4]), s[5]);
    a = __builtin_fmaxf(__builtin_fmaxf(a, s[6]), s[7]); b = __builtin_fmaxf(__builtin_fmaxf(b, s[8]), s[9]); a = __builtin_fmaxf(__builtin_fmaxf(a, s[10]), s[11]);
    b = __builtin_fmaxf(__builtin_fmaxf(b, s[12]), s[13]); a = __builtin_fmaxf(__builtin_fmaxf(a, s[14]), s[15]); return __builtin_fmaxf(a, b); }

struct RtTask { const char* rp; int reo; unsigned rd[8]; };
DI void rt_setup(RtTask& T, const char* wg, const char* wu, const char* wd, int gw) {
    const int lane = lane_id(); const bool isgu = gw < 1296, isd = !isgu && gw < 1872;
    const int span = gw / 36, ks = gw - span * 36, id = isd ? gw - 1296 : 0, rr = lane & 15, gq = lane >> 4, oc = 4 * (rr & 3) + (rr >> 2);
    const int u = id >> 2, s4 = (u * 1821) >> 16, ul = u - 36 * s4, TT = 9 * (8 * s4 + 2 * (ul & 3) + (gq >> 1)) + (ul >> 2);
    T.rp = isgu ? ((ks & 1) ? wu : wg) + ((32 * (ks >> 1) + 8 * gq) * 9216 + span * 256 + rr * 16) : wd + ((128 * (TT >> 3) + (TT & 7) + 32 * (gq & 1)) * 1152 + (id & 3) * 288 + oc * 16);
    T.reo = 256 + rr * 2 - oc * 16; PIN(T.rp); PIN(T.reo);
    const int pa = ks < 24 ? 3 * ks : 48 + ks, pm = ks < 24 ? 1 : 12;
    UNR for (int i = 0; i < 8; ++i) { const int T8 = 8 * span + i, fs = (T8 * 7282) >> 16, t = T8 - fs * 9;
        T.rd[i] = isgu ? WS_WGU + (fs * 324 + (t >= 3 ? 36 * t + ks : pa + pm * t)) * 1024 : WS_WD + ((id & 3) * 144 + (id >> 2)) * 9216 + i * 1024; asm volatile("" : "+s"(T.rd[i])); } }

DI void phase2(const char* x, const RtTask& T, char* ws, P3Pre& P, int w, int head, int qb, int t3, int j3, int xb5, unsigned tlo, unsigned thi KT_ARG) {
    const int lane = lane_id(), r = lane & 31, hh = lane >> 5, kh = w >> 2, gw = blockIdx.x * 8 + w;
    const unsigned lv = lane * 16, lk = kh * K2_HALF + (w & 3) * 1024;
    u32x4 qa, qb4; unsigned qra, qrb;
    { const char* qs = ws + WS_Q + (head * 32 + qb * 4 + (w & 3)) * QIMG; gl16c<0>(qa, lv, qs); gl16c<1024>(qb4, lv, qs); gl4c<2048>(qra, r * 4, qs); gl4c<2176>(qrb, r * 4, qs); }
    const char* kv = ws + WS_KV + head * K2_IMG + lk;
    UNR for (int j = 0; j < 3; ++j) glds16c(lv, kv + j * 4096, lk + j * 4096);
    unsigned ka = kh * K2_HALF + lv, kra = kh * K2_HALF + (hh ? 2304 + 64 : 2048 + r * 8), va = kh * K2_HALF + 2304 + hh * 320 + (r < 18 ? vslot(r) : (r == 18 || r == 22) ? 4 : 5) * 16;
    if (w == 0 && lane == 0) *(LAS unsigned*)(K2_IMG + 7936) = 0u;
    const bool isgu = gw < 1296, isd = !isgu && gw < 1872; const char* rp = T.rp; const int rhi = isgu ? 36864 : 73728, reo = T.reo;
    if (w >= 4) __builtin_amdgcn_s_setprio(1);
    asm volatile("s_waitcnt vmcnt(0) lgkmcnt(0)\n\ts_barrier" : "+v"(qa), "+v"(qb4), "+v"(qra), "+v"(qrb) :: "memory");
    KT_MARK(6);
    const f32x16 z = {};
    f32x16 oa = {}, ob = {}, sa, sb; u32x4 p0, p1 = {0u, 0u, 0u, 0u}, kf = lds128(ka), vf0, vf1 = {0u, 0u, 0u, 0u}; u32x2 kr = lds64(kra), va0, va1, vb0, vb1; float ma, mb;
    { const u32x2 ta = {hh ? 0u : qra, 0u}, tb = {hh ? 0u : qrb, 0u};
      sa = mfma32(kf, qa, z); sb = mfma32(kf, qb4, z); sa = mfma32k8(kr, ta, sa); sb = mfma32k8(kr, tb, sb);
      UNR for (int j = 3; j < 7; ++j) glds16c(lv, kv + j * 4096, lk + j * 4096);
      const unsigned mp = cvtpk(-xhalf_max(max16(sa)), -xhalf_max(max16(sb)));
      ma = -bflo(mp); mb = -bfhi(mp);
      va0 = u32x2{hh ? mp & 0xffffu : qra, 0u}; va1 = u32x2{hh ? mp & 0xffffu : 0u, hh ? 0u : qra}; vb0 = u32x2{hh ? mp >> 16 : qrb, 0u}; vb1 = u32x2{hh ? mp >> 16 : 0u, hh ? 0u : qrb};
      UNR for (int i = 0; i < 16; ++i) sa[i] -= ma; }
#define EX(v) __builtin_amdgcn_exp2f(v)
#define QUAD(S, i, P, j) do { const float e0 = EX(S[i]), e1 = EX(S[i + 1]), e2 = EX(S[i + 2]), e3 = EX(S[i + 3]); P[j] = cvtpk(e0, e1); P[j + 1] = cvtpk(e2, e3); PIN(P); SBAR(); } while (0)
#define STEP(SC, SN, QN, VN, OP, OC, LD, KOFF, VOFF, LDKR, H, H0) do { \
        if (LD) { vf0 = lds128(va + (VOFF)); SBAR(); } \
        SN = mfma32(kf, QN, z); SBAR(); if (LD) kf = lds128(ka + (KOFF)); QUAD(SC, 0, p0, 0); \
        SN = mfma32k8(kr, VN, SN); SBAR(); if (LDKR) kr = lds64(kra + KVIMG); H(IC<H0>{}); QUAD(SC, 4, p0, 2); \
        OP = mfma32(vf1, p1, OP); SBAR(); if (LD) vf1 = lds128(va + (VOFF) + 640); H(IC<H0 + 1>{}); QUAD(SC, 8, p1, 0); \
        OC = mfma32(vf0, p0, OC); SBAR(); H(IC<H0 + 2>{}); QUAD(SC, 12, p1, 2); } while (0)
#define GROUP(H) do { \
        STEP(sa, sb, qb4, vb0, ob, oa, true, 1024, 0, false, H, 0); STEP(sb, sa, qa, va1, oa, ob, false, 0, 0, false, H, 3); \
        STEP(sa, sb, qb4, vb1, ob, oa, true, KVIMG, 1280, true, H, 6); STEP(sb, sa, qa, va0, oa, ob, false, 0, 0, false, H, 9); \
        ka += KVIMG; va += KVIMG; kra += KVIMG; PIN(ka); PIN(va); PIN(kra); } while (0)
    auto none = [&](auto) {};
    Retile R;
    { auto h = [&](auto I) { glds16c(lv, kv + (7 + I) * 4096, lk + (7 + I) * 4096); }; GROUP(h); }
    if (w == 4) poll_dense(ws, WS_FD, lane, tlo, thi);
    { auto h = [&](auto I) { if constexpr (I < 8) { rt_load<I>(R, rp, 9216, rhi); if (isd) rt_load2<I>(R, rp + reo, 9216, rhi); } }; GROUP(h); }
    if (isd) waitv<16>(); else waitv<8>();
    asm volatile("s_barrier" ::: "memory");
    _Pragma("nounroll") for (int gi = 2; gi < 8; ++gi) GROUP(none);
    rt_wait(R);
    { auto h = [&](auto I) { if constexpr (I < 8) { if (isgu || isd) gs16c(lv, ws + T.rd[I], rt_col<I>(R)); } if constexpr (I == 8) { if (isd) gs16c(lv, ws + T.rd[7] + 1024, rt_col9(R)); } }; GROUP(h); }
    { const int kg = w & 3, ng = w >> 2, u0 = kg * 4 + (kg < 2 ? kg : 2), tid = w * 64 + lane;
      const char* wsrc = ws + WS_WO + ((j3 * 18 + u0) * 9 + ng * 4) * 1024; const char* w5 = ng ? ws + WS_WBAR + u0 * 1024 : wsrc + 8192; const int s5 = ng ? 1024 : 9216;
      auto ld = [&](auto N) { constexpr int n = N, i = n / 5, t = n % 5; const int ii = (i == 4 && kg >= 2) ? 3 : i;
          if constexpr (n < 25) { if constexpr (t < 4) gl16c<t * 1024>(P.wf[i][t], lv, wsrc + ii * 9216); else gl16c<0>(P.wf[i][4], lv, w5 + ii * s5); }
          if constexpr (n == 25) gl16<0>(P.xa, r * 1152 + (tid >> 5) * 16, x + (size_t)t3 * 32 * 1152 + j3 * 288);
          if constexpr (n == 26) gl16<256>(P.xb, r * 1152 + hh * 16, x + (size_t)t3 * 32 * 1152 + j3 * 288);
          if constexpr (n == 27) gl4c<0>(P.xm, r * 4, ws + WS_XMEAN + t3 * 128); };
      { auto h = [&](auto I) { ld(I); }; GROUP(h); }
      { auto h = [&](auto I) { ld(IC<12 + I>{}); }; GROUP(h); }
      { waitv<24>(); unsigned old = 0; if (lane == 0) old = __hip_atomic_fetch_add((LAS unsigned*)(K2_IMG + 7936), 1u, __ATOMIC_RELAXED, __HIP_MEMORY_SCOPE_WORKGROUP);
        if (lane == 0 && old == 7u) set_flag(ws, WS_FR + blockIdx.x * 8, tlo, thi); }
#define PRIOG 11
      if (PRIOG == 11) __builtin_amdgcn_s_setprio(0);
      { auto h = [&](auto I) { if constexpr (I < 4) ld(IC<24 + I>{});
                              else if constexpr (I % 2 == 0) gs16c(lv, ws + WS_XB + xb5 * 32768 + (w * 4 + I / 2 - 2) * 1024, u32x4{~0u, ~0u, ~0u, ~0u}); }; GROUP(h); } }
    if (PRIOG == 12) __builtin_amdgcn_s_setprio(0);
    _Pragma("nounroll") for (int gi = 12; gi < 16; ++gi) GROUP(none);
    ob = mfma32(vf1, p1, ob);
    KT_MARK(7);
    { bool bad = !(oa[10] < 1e12f && oa[10] > 1e-6f && ob[10] < 1e12f && ob[10] > 1e-6f);
      UNR for (int i = 0; i < 10; ++i) bad |= !(__builtin_fabsf(oa[i]) < 3e38f && __builtin_fabsf(ob[i]) < 3e38f);
      if (__builtin_expect(__any(bad), 0)) { ka -= K2_HALF; va -= K2_HALF; kra -= K2_HALF; const char* img = ws + WS_KV + head * K2_IMG;
        auto sc = [&](int t, const u32x4& q, const u32x2& t0, const u32x2& t1) { const unsigned cb = (t >> 1) * KVIMG; u32x4 k1; u32x2 k2; gl16c<0>(k1, ka + cb + (t & 1) * 1024, img); gl8c<0>(k2, kra + cb, img);
            asm volatile("s_waitcnt vmcnt(0)" : "+v"(k1), "+v"(k2) :: "memory"); return mfma32k8(k2, (t & 1) ? t1 : t0, mfma32(k1, q, z)); };
        auto redo = [&](const u32x4& q, unsigned q2, f32x16& o, float& m) { const u32x2 t0 = {q2, 0u}, t1 = {0u, q2}; m = -__builtin_inff();
            for (int t = 0; t < 32; ++t) m = __builtin_fmaxf(m, max16(sc(t, q, t0, t1)));
            m = xhalf_max(m); o = z;
            for (int t = 0; t < 32; ++t) { const unsigned cb = (t >> 1) * KVIMG + (t & 1) * 1280; u32x4 v0, v1, a, b; gl16c<0>(v0, va + cb, img); gl16c<640>(v1, va + cb, img);
                const f32x16 s = sc(t, q, t0, t1); asm volatile("" : "+v"(v0), "+v"(v1));
                UNR for (int i = 0; i < 4; ++i) { a[i] = cvtpk(EX(s[2 * i] - m), EX(s[2 * i + 1] - m)); b[i] = cvtpk(EX(s[8 + 2 * i] - m), EX(s[9 + 2 * i] - m)); }
                o = mfma32(v0, a, o); o = mfma32(v1, b, o); } };
        redo(qa, hh ? 0u : va0[0], oa, ma); redo(qb4, hh ? 0u : vb0[0], ob, mb); } }
    auto box = [&](int v) { return (unsigned)((v & 3) < 3 ? (v >> 2) * K2_HALF + (v & 3) * 3072 : K2_IMG + 1024 + (v >> 2) * 3072) + lv; };
    float o[11], mo = kh ? mb : ma;
    { const float ms = kh ? ma : mb; f32x4 sv[3];
      UNR for (int i = 0; i < 11; ++i) { sv[i >> 2][i & 3] = kh ? oa[i] : ob[i]; o[i] = kh ? ob[i] : oa[i]; } sv[2][3] = ms;
      UNR for (int i = 0; i < 3; ++i) *(LAS f32x4*)(box(w) + i * 1024) = sv[i]; }
    lbar();
    { f32x4 rv[3]; UNR for (int i = 0; i < 3; ++i) rv[i] = *(const LAS f32x4*)(box(w ^ 4) + i * 1024);
      const float M = __builtin_fmaxf(mo, rv[2][3]), f0 = EX(mo - M), f1 = EX(rv[2][3] - M);
      UNR for (int i = 0; i < 11; ++i) o[i] = o[i] * f0 + rv[i >> 2][i & 3] * f1; }
    const float il = __builtin_amdgcn_rcpf(o[10]); const int tt = qb * 8 + (w & 3) * 2 + kh;
    gs16c(WS_O + ((tt * 2 + (r >> 4)) * 16 + (head >> 1)) * 1024 + (2 * (head & 1) + hh) * 256 + (r & 15) * 16, ws,
          u32x4{cvtpk(o[0] * il, o[1] * il), cvtpk(o[2] * il, o[3] * il), cvtpk(o[4] * il, o[5] * il), cvtpk(o[6] * il, o[7] * il)});
    if (hh == 0) gs4c(WS_OR + (tt * 32 + head) * 128 + r * 4, ws, cvtpk(o[8] * il, o[9] * il));
#undef STEP
#undef GROUP
#undef QUAD
}

struct WStream { const char* a; const char* b; unsigned lv, lw; };
template <int J> DI void wround(const WStream& S, int w) {
    const char* p = J < 40 ? S.a + J * 8192 : J > 40 ? S.b + (J - 40) * 8192 : (w < 4 ? S.a + J * 8192 : S.b);
    glds16c(S.lv, p, S.lw + (J % 18) * 8192); }

constexpr int K3_ROW = 164 * 4, K3_L = LDS_BYTES - 4 * 32 * K3_ROW;
DI void phase3(char* ws, P3Pre& P, int w, int tt, int j KT_ARG) {
    const int lane = lane_id(), kg = w & 3, ng = w >> 2;
    const unsigned lv = lane * 16;
    const int u0 = kg * 4 + (kg < 2 ? kg : 2);
    u32x4 of[5][2]; unsigned t[16]; f32x4 acc[5][2] = {};
    { const char* os = ws + WS_O + (tt * 32 + u0) * 1024; const int lim = kg < 2 ? 4 : kg == 2 ? 3 : 1;
      if (kg == 3) { const char* rs = ws + WS_OR + tt * 4096; const unsigned ro = (lane >> 4) * 512 + (lane & 15) * 4;
          sfor<16>([&](auto I) { constexpr int i = I; gl4c<(i >> 3) * 2048 + ((i >> 2) & 1) * 64 + (i & 3) * 128>(t[i], ro, rs); }); }
      sfor<5>([&](auto I) { constexpr int i = I; const int ii = i < lim ? i : lim; gl16c<0>(of[i][0], lv, os + ii * 1024); gl16c<0>(of[i][1], lv, os + ii * 1024 + 16384); }); }
    sfor<5>([&](auto I) { constexpr int i = I;
        asm volatile("s_waitcnt vmcnt(%2)" : "+v"(of[i][0]), "+v"(of[i][1]) : "i"(8 - 2 * i) : "memory");
        if constexpr (i == 2 || i == 3) { if (kg == 3) { constexpr int b = (i - 2) * 8;
            asm volatile("" : "+v"(t[b]), "+v"(t[b + 1]), "+v"(t[b + 2]), "+v"(t[b + 3]), "+v"(t[b + 4]), "+v"(t[b + 5]), "+v"(t[b + 6]), "+v"(t[b + 7]));
            of[i][0] = u32x4{t[b], t[b + 1], t[b + 2], t[b + 3]}; of[i][1] = u32x4{t[b + 4], t[b + 5], t[b + 6], t[b + 7]}; } }
        if (i < 4 || kg < 2) { UNR for (int c = 0; c < 5; ++c) { acc[c][0] = mfma16(P.wf[i][c], of[i][0], acc[c][0]); acc[c][1] = mfma16(P.wf[i][c], of[i][1], acc[c][1]); } }
        SBAR(); });
    KT_MARK(11);
    { const int q = lane >> 4;
      UNR for (int b = 0; b < 2; ++b) { const unsigned rowb = K3_L + (kg * 32 + b * 16 + (lane & 15)) * K3_ROW;
          UNR for (int e = 0; e < 4; ++e) *(LAS f32x4*)(rowb + (32 * e + 8 * q + 4 * ng) * 4) = f32x4{acc[0][b][e], acc[1][b][e], acc[2][b][e], acc[3][b][e]};
          *(LAS f32x4*)(rowb + (128 + ng * 16 + 4 * q) * 4) = acc[4][b]; } }
    lbar();
    const unsigned rb = K3_L + (lane & 31) * K3_ROW; float cm = __uint_as_float(P.xm);
    UNR for (int k = 0; k < 4; ++k) { const f32x2 m2 = *(const LAS f32x2*)(rb + k * 32 * K3_ROW + 576); cm += m2[0] + m2[1]; }
    float sq = 0.f;
    auto unit = [&](unsigned col, const u32x4& xr, unsigned dst) { f32x4 a = {}, b = {};
        UNR for (int k = 0; k < 4; ++k) { a += *(const LAS f32x4*)(rb + k * 32 * K3_ROW + col); b += *(const LAS f32x4*)(rb + k * 32 * K3_ROW + col + 16); }
        const u32x4 hv = {cvtpk(a[0] + bflo(xr[0]) - cm, a[1] + bfhi(xr[0]) - cm), cvtpk(a[2] + bflo(xr[1]) - cm, a[3] + bfhi(xr[1]) - cm),
                          cvtpk(b[0] + bflo(xr[2]) - cm, b[1] + bfhi(xr[2]) - cm), cvtpk(b[2] + bflo(xr[3]) - cm, b[3] + bfhi(xr[3]) - cm)};
        gs16c(dst, ws, hv);
        UNR for (int e = 0; e < 4; ++e) { sq = fmaf(bflo(hv[e]), bflo(hv[e]), sq); sq = fmaf(bfhi(hv[e]), bfhi(hv[e]), sq); } };
    const unsigned dst = WS_HC + (tt * 2 + ((lane >> 4) & 1)) * 18432 + (18 * j + (lane >> 5)) * 256 + (lane & 15) * 16;
    unit((2 * w + (lane >> 5)) * 32, P.xa, dst + w * 512);
    if (w == 7) unit(512 + (lane >> 5) * 32, P.xb, dst + 4096);
    *(LAS float*)(73728 + (w * 64 + lane) * 4) = sq;
    if (w == 6 && j == 0 && lane < 32) gs4c(WS_CM + tt * 128 + lane * 4, ws, __float_as_uint(cm));
    lbar();
    if (w == 5 && lane < 32) { float t2 = 0.f; UNR for (int i = 0; i < 16; ++i) t2 += *(const LAS float*)(73728 + i * 128 + lane * 4);
        gs4c(WS_SSQ + (tt * 4 + j) * 128 + lane * 4, ws, __float_as_uint(t2)); }
}

constexpr int FPF = 5, K4_X0 = 4, K5_AW = 4, NSLOT = 486;
HD constexpr int k4_tile(int q) { return q < 72 ? q % 3 : q < 108 ? (q - 72) / 12 : q / 36; }
HD constexpr int k4_ks(int q) { return q < 72 ? q / 3 : q < 108 ? 24 + (q - 72) % 12 : q % 36; }
HD constexpr bool f_bar(int g) { return g < 324 ? (g > 0 && g % 24 == 0) : (g - 324) % 18 == 0; }
struct FSched { int wn[NSLOT + 1], ln[NSLOT + 1], wslot[81], lslot[72]; };
HD constexpr FSched f_make() { FSched F{}; int wn = 18, ln = K4_X0, cons = 0;
    for (int j = 0; j < 18; ++j) F.wslot[j] = -2; for (int i = 0; i < K4_X0; ++i) F.lslot[i] = -1;
    for (int g = 0; g < NSLOT; ++g) { F.wn[g] = wn; F.ln[g] = ln;
        if (f_bar(g)) cons = g < 324 ? g : 324 + 2 * (g - 324);
        if ((g < 324 ? (g >= 60 && g % 4 == 2) : g % 3 == 1) && wn < 81 && 8 * wn + 8 <= cons + 144) F.wslot[wn++] = g;
        if (g % 2 == 1) { if (ln < 36) F.lslot[ln++] = g;
            else if (ln < 72 && g > 312) { const int pp = (ln - 36) / 2, cur = g < 324 ? 0 : (g - 324) / 9;
                if (pp < cur + K5_AW && (pp < 10 || g >= 378) && (pp < 14 || g >= 414)) F.lslot[ln++] = g; } } }
    F.wn[NSLOT] = wn; F.ln[NSLOT] = ln; return F; }
constexpr FSched FS = f_make();
HD constexpr int f_after(int slot, int g) { return (FS.wn[g] - FS.wn[slot + 1]) + (FS.ln[g] - FS.ln[slot + 1]); }
HD constexpr int k4_epi_start(int e) { return e == 0 ? 86 : e == 8 ? 327 : 108 + 31 * (e - 1); }
HD constexpr int k4_epi_len(int e) { return e == 0 ? 22 : e == 7 ? 30 : e == 8 ? 18 : 31; }
HD constexpr int k4_epi_first(int e, int gap) { const int g = gap - 2 * k4_epi_start(e), n = 2 * k4_epi_len(e); return g <= 0 ? 0 : g >= n ? 70 : (g * 70 + n - 1) / n; }
HD constexpr int f_barwait(int g) {
    int nb = g + 1; while (nb < NSLOT && !f_bar(nb)) ++nb;
    const int lim = nb < 324 ? nb + FPF : 324 + 2 * (nb - 324) + 18; int need = (lim + 7) / 8 - 1; if (need > 80) need = 80;
    if (need >= FS.wn[g]) return -1;
    int n = (FS.wn[g] - need - 1) + (FS.ln[g] - (need < 18 ? 0 : FS.ln[FS.wslot[need]]));
    const int e = g == 264 ? 4 : g == 324 ? 6 : g == 360 ? 8 : -1;
    if (e >= 0) { const int m = f_after(k4_epi_start(e) + k4_epi_len(e) - 1, g); if (m < n) n = m; }
    return n > 63 ? 63 : n; }

DI void phase45(char* ws, char* out, const WStream& S, int w, int qb, int fs, int m128, int j, int s4, unsigned tlo, unsigned thi KT_ARG) {
    const int lane = lane_id(), tt = qb * 8 + w, tq = w & 3, kg = w >> 2;
    const unsigned lv = lane * 16;
    unsigned sq[8]; u32x4 xf[36], af[K5_AW][2];
    const char* xs = ws + WS_HC + tt * 36 * 1024;
    const char* as = ws + WS_A + ((m128 * 8 + tq * 2) * 144 + s4 * 36 + kg) * 1024;
    auto ldr = [&](auto K) { constexpr int k = K;
        if constexpr (k < 36) gl16c<((k >> 1) % 3) * 1024>(xf[k], lv, xs + ((k & 1) * 18 + (k >> 1) / 3 * 3) * 1024);
        else { constexpr int i = k - 36, p = i / 2; gl16c<0>(af[p % K5_AW][i & 1], lv, as + p * 2048 + (i & 1) * 147456); } };
    sfor<K4_X0>(ldr);
    { const char* sp = ws + WS_SSQ + tt * 512; sfor<8>([&](auto I) { gl4c<I * 64>(sq[I], (lane & 15) * 4, sp); }); }
    float A1[2], A3[2], IR2[2];
    unsigned adst = WS_A + ((tt * 2 + (lane >> 5)) * 144) * 1024 + (lane & 31) * 16;
    char* abase = ws + ((fs >> 3) * 36 + ((fs >> 1) & 3)) * 1024 + (fs & 1) * 512;
    unsigned la = lv, lb = lv + 49152, lc = lv + 98304; PIN(la); PIN(lb); PIN(lc);
    unsigned ld, le, lf; u32x2 fv;
    f32x4 acc4[3][4], acc[9][2]; u32x4 wf[FPF]; unsigned pk[4]; float ez[2], ew[2];
    auto rdw = [&](auto G) { constexpr int g = G;
        if constexpr (g < 324) { constexpr int s = g % 144; wf[g % FPF] = lds128((s < 48 ? la : s < 96 ? lb : lc) + (s % 48) * 1024); }
        else { constexpr int q = g - 324, s = (36 + 18 * (q / 9) + q % 9) % 144; wf[g % FPF] = lds128((s < 54 ? ld : s < 108 ? le : lf) + (s % 54) * 1024); } };
    auto epi = [&](auto E, auto I) { constexpr int e = E, i = I, pr = i / 17, st = i % 17, b = pr >> 1, h = st & 1, c = (pr & 1) * 2 + h; f32x4 (&a)[4] = acc4[e % 3];
        if constexpr (i < 68) {
            if constexpr (st < 2) ez[h] = a[b][c] * a[b][c];
            else if constexpr (st < 4) ez[h] = fmaf(ez[h], A3[b], A1[b]);
            else if constexpr (st < 6) ez[h] *= a[b][c];
            else if constexpr (st < 8) ez[h] = __builtin_amdgcn_exp2f(ez[h]);
            else if constexpr (st < 10) ew[h] = a[b][c] * a[2 + b][c];
            else if constexpr (st < 12) ez[h] = fmaf(ez[h], IR2[b], IR2[b]);
            else if constexpr (st < 14) ez[h] = __builtin_amdgcn_rcpf(ez[h]);
            else if constexpr (st < 16) ew[h] *= ez[h];
            else pk[pr] = cvtpk(ew[0], ew[1]);
            if constexpr (st == 16) PIN(pk[pr]); else if constexpr (st == 8 || st == 9 || st >= 14) PIN(ew[h]); else PIN(ez[h]);
        } else if constexpr (i == 68) { const auto r0 = __builtin_amdgcn_permlane32_swap(pk[0], pk[2], false, false); pk[0] = r0[0]; pk[2] = r0[1]; PIN(pk[0]); PIN(pk[2]); }
        else { const auto r1 = __builtin_amdgcn_permlane32_swap(pk[1], pk[3], false, false); gs16c(adst, abase + e * 4096, u32x4{pk[0], r1[0], pk[2], r1[1]}); } };
    auto gap = [&](auto G) { sfor<9>([&](auto E) { constexpr int f = k4_epi_first(E, G), l = k4_epi_first(E, G + 1); sfor<l - f>([&](auto I) { epi(E, IC<f + I>{}); }); }); SBAR(); };
    sfor<FPF - 1>([&](auto I) { rdw(I); });
    sfor<NSLOT>([&](auto G) { constexpr int g = G;
        constexpr int stg = g == 312 || g == 300 ? 0 : g == 378 || g == 366 ? 1 : g == 414 || g == 402 ? 2 : -1;
        if constexpr (f_bar(g)) {
            if constexpr (stg >= 0) { if (w == 1) { asm volatile("s_waitcnt vmcnt(%1)" : "+v"(fv) : "i"(f_after(g - 13, g)) : "memory");
                if (!__all(fv[0] == tlo && fv[1] == thi)) poll_flags(ws, WS_F4 + (qb * 32 + s4 * 8 + (lane & 7)) * 128 + stg * 8, true, tlo, thi); } }
            static_assert(f_barwait(g) >= 0, "needed round not issued");
            waitv_bar<f_barwait(g)>();
            if constexpr (g == 264 || g == 324 || g == 360) { if (w == 0 && lane == 0) set_flag(ws, WS_F4 + (qb * 32 + fs) * 128 + (g == 264 ? 0 : g == 324 ? 8 : 16), tlo, thi); }
        } else if constexpr (stg >= 0) { if (w == 1) gl8c<stg * 8>(fv, WS_F4 + (qb * 32 + s4 * 8 + (lane & 7)) * 128, ws); }
        if constexpr (FS.wn[g + 1] > FS.wn[g]) wround<FS.wn[g]>(S, w);
        if constexpr (FS.ln[g + 1] > FS.ln[g]) ldr(IC<FS.ln[g]>{});
        if constexpr (g == 108) KT_MARK(14); if constexpr (g == 216) KT_MARK(15); if constexpr (g == 325) { KT_MARK(16); KT_MARK(17); KT_MARK(18); } if constexpr (g == 405) KT_MARK(19);
        if constexpr (g == 316) { ld = lv + kg * 9216; le = ld + 54 * 1024; lf = ld + 108 * 1024; PIN(ld); PIN(le); PIN(lf); }
        if constexpr (g < 324) { constexpr int t = k4_tile(g), ks = k4_ks(g), x0 = ks & ~1, m = ks & 1;
            if constexpr (t == 0 && g < 84 && m == 0) { static_assert(FS.ln[g + 1] >= ks + 2, "X not issued");
                constexpr int n = (FS.ln[g + 1] - ks - 2) + (FS.wn[g + 1] - FS.wn[FS.lslot[ks + 1] + 1]) + (ks + 1 < K4_X0 ? 8 : 0);
                asm volatile("s_waitcnt vmcnt(%2)" : "+v"(xf[ks]), "+v"(xf[ks + 1]) : "i"(n) : "memory"); }
            if constexpr (ks < 2) mfma16z(acc4[t % 3][2 * m], wf[g % FPF], xf[x0]); else mfma16a(acc4[t % 3][2 * m], wf[g % FPF], xf[x0]);
            SBAR();
            if constexpr (g == 40) { asm volatile("" : "+v"(sq[0]), "+v"(sq[1]), "+v"(sq[2]), "+v"(sq[3]), "+v"(sq[4]), "+v"(sq[5]), "+v"(sq[6]), "+v"(sq[7]));
                UNR for (int b = 0; b < 2; ++b) {
                    const float var = ((__uint_as_float(sq[b]) + __uint_as_float(sq[2 + b])) + (__uint_as_float(sq[4 + b]) + __uint_as_float(sq[6 + b]))) * (1.f / 576.f) + 1e-5f;
                    const float rs = __builtin_amdgcn_rsqf(var);
                    A1[b] = -2.f * 0.7978845608028654f * 1.4426950408889634f * rs; A3[b] = A1[b] * 0.044715f * rs * rs; IR2[b] = var; PIN(A1[b]); PIN(A3[b]); PIN(IR2[b]); } }
            gap(IC<2 * g>{}); rdw(IC<g + FPF - 1>{}); SBAR();
            if constexpr (ks < 2) mfma16z(acc4[t % 3][2 * m + 1], wf[g % FPF], xf[x0 + 1]); else mfma16a(acc4[t % 3][2 * m + 1], wf[g % FPF], xf[x0 + 1]);
            SBAR();
            gap(IC<2 * g + 1>{});
        } else { constexpr int q = g - 324, p = q / 9, T = q % 9;
            if constexpr (T == 0) { constexpr int i = 36 + 2 * p + 1; static_assert(FS.ln[g + 1] > i, "a not issued");
                constexpr int n = (FS.ln[g + 1] - i - 1) + (FS.wn[g + 1] - FS.wn[FS.lslot[i] + 1]);
                asm volatile("s_waitcnt vmcnt(%2)" : "+v"(af[p % K5_AW][0]), "+v"(af[p % K5_AW][1]) : "i"(n) : "memory"); }
            if constexpr (p == 0) mfma16z(acc[T][0], wf[g % FPF], af[0][0]); else mfma16a(acc[T][0], wf[g % FPF], af[p % K5_AW][0]);
            SBAR();
            gap(IC<2 * g>{}); if constexpr (g + FPF - 1 < NSLOT) rdw(IC<g + FPF - 1>{}); SBAR();
            if constexpr (p == 0) mfma16z(acc[T][1], wf[g % FPF], af[0][1]); else mfma16a(acc[T][1], wf[g % FPF], af[p % K5_AW][1]);
            SBAR();
            gap(IC<2 * g + 1>{}); }
    });
    asm volatile("s_nop 7" : "+v"(acc[7][0]), "+v"(acc[7][1]), "+v"(acc[8][0]), "+v"(acc[8][1]));
    KT_MARK(20);
    const int q = lane >> 4, gt = m128 * 128 + tq * 32 + kg * 16 + (lane & 15), grp = m128 * 4 + j;
    u32x4 hr[4]; u32x2 h9; unsigned cmu;
    if (tq == s4) { const char* hs = ws + WS_HC + (m128 * 8 + tq * 2 + kg) * 18432 + j * 4608;
        gl16c<0>(hr[0], lv, hs); gl16c<1024>(hr[1], lv, hs); gl16c<2048>(hr[2], lv, hs); gl16c<3072>(hr[3], lv, hs);
        gl8c<0>(h9, (q >> 1) * 256 + (lane & 15) * 16 + (q & 1) * 8, hs + 4096); gl4c<0>(cmu, gt * 4, ws + WS_CM); }
    UNR for (int t = 0; t < 9; ++t) *(LAS f32x4*)(73728 + w * 9216 + t * 1024 + lv) = kg ? acc[t][0] : acc[t][1];
    lbar();
    f32x4 v[9];
    UNR for (int t = 0; t < 9; ++t) v[t] = (kg ? acc[t][1] : acc[t][0]) + *(const LAS f32x4*)(73728 + (w ^ 4) * 9216 + t * 1024 + lv);
    KT_MARK(21);
    if (tq != s4) { char* dst = ws + WS_XB + (grp * 4 + tq) * 32768 + (((s4 - tq - 1) & 3) * 2 + kg) * 4608;
        UNR for (int e = 0; e < 4; ++e) gs16c(lv + e * 1024, dst, u32x4{cvtpk(v[0][e], v[1][e]), cvtpk(v[2][e], v[3][e]), cvtpk(v[4][e], v[5][e]), cvtpk(v[6][e], v[7][e])});
        gs8c(4096 + lane * 8, dst, u32x2{cvtpk(v[8][0], v[8][1]), cvtpk(v[8][2], v[8][3])});
        if (lane == 0) gs4c(WS_XB + (grp * 4 + tq) * 32768 + 27648 + (((s4 - tq - 1) & 3) * 2 + kg) * 128, ws, 0u);
    } else {
        asm volatile("s_waitcnt vmcnt(0)" : "+v"(hr[0]), "+v"(hr[1]), "+v"(hr[2]), "+v"(hr[3]), "+v"(h9), "+v"(cmu) :: "memory");
        const float cm = __uint_as_float(cmu);
        UNR for (int e = 0; e < 4; ++e) { UNR for (int i = 0; i < 4; ++i) { v[2 * i][e] += bflo(hr[e][i]) + cm; v[2 * i + 1][e] += bfhi(hr[e][i]) + cm; } }
        v[8][0] += bflo(h9[0]) + cm; v[8][1] += bfhi(h9[0]) + cm; v[8][2] += bflo(h9[1]) + cm; v[8][3] += bfhi(h9[1]) + cm;
        UNR for (int t = 0; t < 9; ++t) PIN(v[t]);
        for (unsigned spins = 0; spins < (1u << 20); ++spins) { unsigned hv; gl4c<0>(hv, WS_XB + (grp * 4 + s4) * 32768 + 27648 + ((lane < 3 ? lane : 0) * 2 + kg) * 128, ws);
            asm volatile("s_waitcnt vmcnt(0)" : "+v"(hv) :: "memory"); if (__all(hv == 0u)) break; }
        u32x4 pa[3][4]; u32x2 pb[3]; const char* src = ws + WS_XB + (grp * 4 + s4) * 32768 + kg * 4608;
        for (unsigned spins = 0; spins < (1u << 16); ++spins) {
            sfor<3>([&](auto D) { constexpr int d = D;
                gl16c<0>(pa[d][0], lv, src + d * 9216); gl16c<1024>(pa[d][1], lv, src + d * 9216); gl16c<2048>(pa[d][2], lv, src + d * 9216); gl16c<3072>(pa[d][3], lv, src + d * 9216); gl8c<0>(pb[d], lane * 8, src + d * 9216 + 4096); });
            asm volatile("s_waitcnt vmcnt(0)" : "+v"(pa[0][0]), "+v"(pa[0][1]), "+v"(pa[0][2]), "+v"(pa[0][3]), "+v"(pa[1][0]), "+v"(pa[1][1]), "+v"(pa[1][2]), "+v"(pa[1][3]),
                         "+v"(pa[2][0]), "+v"(pa[2][1]), "+v"(pa[2][2]), "+v"(pa[2][3]), "+v"(pb[0]), "+v"(pb[1]), "+v"(pb[2]) :: "memory");
            unsigned mx = 0;
            UNR for (int d = 0; d < 3; ++d) { UNR for (int e = 0; e < 4; ++e) UNR for (int i = 0; i < 4; ++i) mx = mx > pa[d][e][i] ? mx : pa[d][e][i];
                mx = mx > pb[d][0] ? mx : pb[d][0]; mx = mx > pb[d][1] ? mx : pb[d][1]; }
            if (__all(mx != ~0u)) break; }
        UNR for (int d = 0; d < 3; ++d) { UNR for (int e = 0; e < 4; ++e) UNR for (int i = 0; i < 4; ++i) { v[2 * i][e] += bflo(pa[d][e][i]); v[2 * i + 1][e] += bfhi(pa[d][e][i]); }
            v[8][0] += bflo(pb[d][0]); v[8][1] += bfhi(pb[d][0]); v[8][2] += bflo(pb[d][1]); v[8][3] += bfhi(pb[d][1]); }
        char* od = out + (size_t)gt * 1152 + j * 288;
        UNR for (int e = 0; e < 4; ++e) *(u32x4*)(od + e * 64 + q * 16) = u32x4{cvtpk(v[0][e], v[1][e]), cvtpk(v[2][e], v[3][e]), cvtpk(v[4][e], v[5][e]), cvtpk(v[6][e], v[7][e])};
        *(u32x2*)(od + 256 + q * 8) = u32x2{cvtpk(v[8][0], v[8][1]), cvtpk(v[8][2], v[8][3])};
    }
}

#define KT_FWD

__global__ __launch_bounds__(512, 2) void block_fwd(const char* __restrict__ x, const char* __restrict__ wqkv, const char* __restrict__ wo, const char* __restrict__ wg,
                                                    const char* __restrict__ wu, const char* __restrict__ wd, char* __restrict__ ws, char* __restrict__ out KT_ARG) {
    extern __shared__ __attribute__((aligned(16))) char smem[];
    const int w = rfl(threadIdx.x >> 6), xc = blockIdx.x & 7, rk = blockIdx.x >> 3, hs = rk & 3, qb = rk >> 2;
    KT_MARK(0);
    u32x2 ep; gl8c<0>(ep, WS_EPOCH, ws);
    { const char* pc0; unsigned clen;
      asm volatile("s_getpc_b64 %0\n.Lblk_begin:\n\ts_mov_b32 %1, .Lblk_end-.Lblk_begin" : "=s"(pc0), "=s"(clen));
      if (w == 7) { const unsigned o = (rk * 16 + (lane_id() & 15)) * 128; touch(o < clen ? o : 0u, pc0, 163328); } }
    const int mt1 = (xc >> 1) * 8 + (rk & 7), ct1 = (xc & 1) * 4 + (rk >> 3);
    phase1(x, wqkv, wo, ws, w, mt1, ct1 KT_FWD);
    asm volatile("s_waitcnt vmcnt(0) lgkmcnt(0)\n\ts_barrier" : "+v"(ep) :: "memory");
    KT_MARK(4);
    const u64 tag = (((u64)rfl(ep[1]) << 32) | rfl(ep[0])) + 1; const unsigned tlo = (unsigned)tag, thi = (unsigned)(tag >> 32);
    RtTask RT;
    if (w == 0) { const int lane = lane_id(), pp = xc * 2 + (hs >> 1);
        if (lane < 2) set_flag(ws, lane ? WS_FD + blockIdx.x * 8 : WS_F1 + (ct1 * 32 + mt1) * 128, tlo, thi);
        rt_setup(RT, wg, wu, wd, blockIdx.x * 8);
        poll_flags2(ws, WS_F1 + (((lane < 32 ? 16 : 32) + pp) / 6 * 32 + (lane & 31)) * 128, WS_F1 + (pp / 6 * 32 + qb * 4 + (lane & 3)) * 128, tlo, thi); }
    else rt_setup(RT, wg, wu, wd, blockIdx.x * 8 + w);
    asm volatile("s_barrier" ::: "memory");
    KT_MARK(5);
    P3Pre P;
    const int mt4 = qb, fs4 = xc * 4 + hs, m5 = qb * 2 + (xc & 1), j5 = hs, s5 = xc >> 1;
    phase2(x, RT, ws, P, w, xc * 4 + hs, qb, qb * 8 + xc, hs, (m5 * 4 + j5) * 4 + s5, tlo, thi KT_FWD);
    if (w == 0) poll_dense(ws, WS_FR, lane_id(), tlo, thi);
    KT_MARK(8);
    fullbar();
    UNR for (int i = 0; i < 5; ++i) asm volatile("" : "+v"(P.wf[i][0]), "+v"(P.wf[i][1]), "+v"(P.wf[i][2]), "+v"(P.wf[i][3]), "+v"(P.wf[i][4]));
    asm volatile("" : "+v"(P.xa), "+v"(P.xb), "+v"(P.xm));
    KT_MARK(9);
    if (w == 0 && lane_id() == 0) set_flag(ws, WS_F2 + (qb * 32 + xc * 4 + hs) * 128, tlo, thi);
    WStream S; S.lv = lane_id() * 16; S.lw = w * 1024;
    S.a = ws + WS_WGU + fs4 * 324 * 1024 + w * 1024;
    S.b = ws + WS_WD + (j5 * 144 + s5 * 36) * 9216 + w * 1024 - 4096;
    sfor<9>([&](auto J) { wround<J>(S, w); });
    if (w == 0) poll_flags(ws, WS_F2 + (qb * 32 + (lane_id() & 31)) * 128, true, tlo, thi);
    asm volatile("s_barrier" ::: "memory");
    KT_MARK(10);
    phase3(ws, P, w, qb * 8 + xc, hs KT_FWD);
    fullbar();
    KT_MARK(12);
    if (w == 0 && lane_id() == 0) set_flag(ws, WS_F3 + (qb * 32 + xc * 4 + hs) * 128, tlo, thi);
    sfor<9>([&](auto J) { wround<9 + J>(S, w); });
    if (w == 0) poll_flags(ws, WS_F3 + (mt4 * 32 + (lane_id() & 31)) * 128, true, tlo, thi);
    asm volatile("s_barrier" ::: "memory");
    KT_MARK(13);
    phase45(ws, out, S, w, qb, fs4, m5, j5, s5, tlo, thi KT_FWD);
    asm volatile(".Lblk_end:");
    if (blockIdx.x == 0 && w == 0 && lane_id() == 0) set_flag(ws, WS_EPOCH, tlo, thi);
    KT_MARK(22);
}

#define KT_PASS

extern "C" void kernel_launch(void* const* d_in, const int* in_sizes, int n_in, void* d_out, int out_size, void* d_ws, size_t ws_size, hipStream_t stream) {
    (void)hipFuncSetAttribute((const void*)block_fwd, hipFuncAttributeMaxDynamicSharedMemorySize, LDS_BYTES);
    block_fwd<<<256, 512, LDS_BYTES, stream>>>((const char*)d_in[0], (const char*)d_in[1], (const char*)d_in[2], (const char*)d_in[4], (const char*)d_in[5], (const char*)d_in[6],
                                                (char*)d_ws, (char*)d_out KT_PASS);
}
```

```cpp
#include <hip/hip_runtime.h>
#include <hip/hip_bf16.h>
#include <stdint.h>
#include <utility>

typedef float f32x16 __attribute__((ext_vector_type(16)));
typedef float f32x4 __attribute__((ext_vector_type(4)));
typedef float f32x2 __attribute__((ext_vector_type(2)));
typedef __bf16 bf16x8 __attribute__((ext_vector_type(8)));
typedef __bf16 bf16x2 __attribute__((ext_vector_type(2)));
typedef short s16x4 __attribute__((ext_vector_type(4)));
typedef unsigned u32x4 __attribute__((ext_vector_type(4)));
typedef unsigned u32x2 __attribute__((ext_vector_type(2)));
typedef unsigned long long u64;
#define LAS __attribute__((address_space(3)))
#define GAS __attribute__((address_space(1)))
#define DI __device__ __forceinline__
#define HD __host__ __device__
#define SBAR() __builtin_amdgcn_sched_barrier(0)
#define PIN(x) asm volatile("" : "+v"(x))
#define UNR _Pragma("unroll")
template <int V> using IC = std::integral_constant<int, V>;

constexpr unsigned QIMG = 2304, KVIMG = 4864;
constexpr unsigned WS_EPOCH = 0;
constexpr unsigned WS_FR = 256, WS_FD = WS_FR + 2048;
constexpr unsigned WS_F1 = 8192, WS_F2 = WS_F1 + 262144, WS_F3 = WS_F2 + 262144, WS_F4 = WS_F3 + 262144;
constexpr unsigned WS_XMEAN = WS_F4 + 32768;
constexpr unsigned WS_CM = WS_XMEAN + 8192;
constexpr unsigned WS_SSQ = WS_CM + 8192;
constexpr unsigned WS_WBAR = WS_SSQ + 32768;
constexpr unsigned WS_Q = WS_WBAR + 18432;
constexpr unsigned WS_KV = WS_Q + 32 * 32 * QIMG;
constexpr unsigned WS_O = WS_KV + 32 * 32 * KVIMG;
constexpr unsigned WS_OR = WS_O + 128 * 16 * 1024;
constexpr unsigned WS_HC = WS_OR + 64 * 32 * 128;
constexpr unsigned WS_WO = WS_HC + 64 * 36 * 1024;
constexpr unsigned WS_WGU = WS_WO + 4 * 18 * 9 * 1024;
constexpr unsigned WS_WD = WS_WGU + 32 * 324 * 1024;
constexpr unsigned WS_A = WS_WD + 4 * 144 * 9 * 1024;
constexpr unsigned WS_XB = WS_A + 128 * 144 * 1024;
constexpr unsigned WS_END = WS_XB + 256 * 32768;
constexpr int LDS_BYTES = 163840;

DI unsigned rfl(unsigned x) { return __builtin_amdgcn_readfirstlane(x); }
DI unsigned cvtpk(float lo, float hi) { f32x2 v = {lo, hi}; bf16x2 b = __builtin_convertvector(v, bf16x2); return __builtin_bit_cast(unsigned, b); }
DI float bflo(unsigned u) { return __uint_as_float(u << 16); }
DI float bfhi(unsigned u) { return __uint_as_float(u & 0xffff0000u); }
DI float dot2(unsigned a, unsigned b, float c) { return __builtin_amdgcn_fdot2_f32_bf16(__builtin_bit_cast(bf16x2, a), __builtin_bit_cast(bf16x2, b), c, false); }
DI u32x4 addc8(const u32x4& v, unsigned lo, unsigned hi, float c) { float t0, t1, t2, t3, t4, t5, t6, t7;
    asm("v_dot2_f32_bf16 %0, %8, %12, %14\n\tv_dot2_f32_bf16 %1, %8, %13, %14\n\tv_dot2_f32_bf16 %2, %9, %12, %14\n\tv_dot2_f32_bf16 %3, %9, %13, %14\n\t"
        "v_dot2_f32_bf16 %4, %10, %12, %14\n\tv_dot2_f32_bf16 %5, %10, %13, %14\n\tv_dot2_f32_bf16 %6, %11, %12, %14\n\tv_dot2_f32_bf16 %7, %11, %13, %14\n\ts_nop 2"
        : "=&v"(t0), "=&v"(t1), "=&v"(t2), "=&v"(t3), "=&v"(t4), "=&v"(t5), "=&v"(t6), "=&v"(t7) : "v"(v[0]), "v"(v[1]), "v"(v[2]), "v"(v[3]), "v"(lo), "v"(hi), "v"(c));
    return u32x4{cvtpk(t0, t1), cvtpk(t2, t3), cvtpk(t4, t5), cvtpk(t6, t7)}; }
DI f32x16 mfma32(u32x4 a, u32x4 b, f32x16 c) { return __builtin_amdgcn_mfma_f32_32x32x16_bf16(__builtin_bit_cast(bf16x8, a), __builtin_bit_cast(bf16x8, b), c, 0, 0, 0); }
DI f32x16 mfma32k8(u32x2 a, u32x2 b, f32x16 c) { return __builtin_amdgcn_mfma_f32_32x32x8bf16_1k(__builtin_bit_cast(s16x4, a), __builtin_bit_cast(s16x4, b), c, 0, 0, 0); }
DI f32x4 mfma16(u32x4 a, u32x4 b, f32x4 c) { return __builtin_amdgcn_mfma_f32_16x16x32_bf16(__builtin_bit_cast(bf16x8, a), __builtin_bit_cast(bf16x8, b), c, 0, 0, 0); }
DI void mfma16a(f32x4& c, const u32x4& a, const u32x4& b) { asm volatile("v_mfma_f32_16x16x32_bf16 %0, %1, %2, %0" : "+v"(c) : "v"(a), "v"(b)); }
DI void mfma16z(f32x4& c, const u32x4& a, const u32x4& b) { asm volatile("v_mfma_f32_16x16x32_bf16 %0, %1, %2, 0" : "=&v"(c) : "v"(a), "v"(b)); }
DI void glds16(unsigned voff, const void* sb, unsigned lds) {
    asm volatile("s_mov_b32 m0, %2\n\ts_nop 0\n\tglobal_load_lds_dwordx4 %0, %1" :: "v"(voff), "s"(sb), "s"(lds) : "memory"); }
template <int LDS> DI void glds16i(unsigned voff, const void* sb) {
    asm volatile("s_mov_b32 m0, %2\n\ts_nop 0\n\tglobal_load_lds_dwordx4 %0, %1" :: "v"(voff), "s"(sb), "i"(LDS) : "memory"); }
DI void glds16c(unsigned voff, const void* sb, unsigned lds) {
    asm volatile("s_mov_b32 m0, %2\n\ts_nop 0\n\tglobal_load_lds_dwordx4 %0, %1 sc1" :: "v"(voff), "s"(sb), "s"(lds) : "memory"); }
DI void touch(unsigned voff, const void* sb, unsigned lds) {
    asm volatile("s_mov_b32 m0, %2\n\ts_nop 0\n\tglobal_load_lds_dword %0, %1" :: "v"(voff), "s"(sb), "s"(lds) : "memory"); }
template <int OFF> DI void gl16(u32x4& d, unsigned voff, const void* sb) { asm volatile("global_load_dwordx4 %0, %1, %2 offset:%3" : "=v"(d) : "v"(voff), "s"(sb), "i"(OFF) : "memory"); }
template <int OFF> DI void gl16c(u32x4& d, unsigned voff, const void* sb) { asm volatile("global_load_dwordx4 %0, %1, %2 offset:%3 sc1" : "=v"(d) : "v"(voff), "s"(sb), "i"(OFF) : "memory"); }
template <int OFF> DI void gl8c(u32x2& d, unsigned voff, const void* sb) { asm volatile("global_load_dwordx2 %0, %1, %2 offset:%3 sc1" : "=v"(d) : "v"(voff), "s"(sb), "i"(OFF) : "memory"); }
template <int OFF> DI void gl4c(unsigned& d, unsigned voff, const void* sb) { asm volatile("global_load_dword %0, %1, %2 offset:%3 sc1" : "=v"(d) : "v"(voff), "s"(sb), "i"(OFF) : "memory"); }
DI void gl2(unsigned& d, unsigned voff, const void* sb) { asm volatile("global_load_ushort %0, %1, %2" : "=v"(d) : "v"(voff), "s"(sb) : "memory"); }
DI void gs16c(unsigned voff, void* sb, u32x4 v) { asm volatile("global_store_dwordx4 %0, %1, %2 sc1\n\ts_nop 1" :: "v"(voff), "v"(v), "s"(sb) : "memory"); }
DI void gs8c(unsigned voff, void* sb, u32x2 v) { asm volatile("global_store_dwordx2 %0, %1, %2 sc1" :: "v"(voff), "v"(v), "s"(sb) : "memory"); }
DI void gs4c(unsigned voff, void* sb, unsigned v) { asm volatile("global_store_dword %0, %1, %2 sc1" :: "v"(voff), "v"(v), "s"(sb) : "memory"); }
template <int N> DI void waitv() { asm volatile("s_waitcnt vmcnt(%0)" :: "i"(N) : "memory"); }
template <int N> DI void waitv_bar() { asm volatile("s_waitcnt vmcnt(%0)\n\ts_barrier" :: "i"(N) : "memory"); }
DI void lbar() { asm volatile("s_waitcnt lgkmcnt(0)\n\ts_barrier" ::: "memory"); }
DI void fullbar() { asm volatile("s_waitcnt vmcnt(0) lgkmcnt(0)\n\ts_barrier" ::: "memory"); }
DI u32x4 lds128(unsigned a) { return *(const LAS u32x4*)(a); }
DI u32x2 lds64(unsigned a) { return *(const LAS u32x2*)(a); }
DI void sts128(unsigned a, u32x4 v) { *(LAS u32x4*)(a) = v; }
DI u32x2 ldstr(unsigned a) { return __builtin_bit_cast(u32x2, __builtin_amdgcn_ds_read_tr16_b64_v4i16((LAS s16x4*)(a))); }
template <int CTRL> DI float dpp_add(float v) { return v + __uint_as_float(__builtin_amdgcn_update_dpp(0u, __float_as_uint(v), CTRL, 0xf, 0xf, true)); }
DI float red8(float v) { v = dpp_add<0xB1>(v); v = dpp_add<0x4E>(v); return dpp_add<0x141>(v); }
template <class F, int... Is> DI void sfor_impl(F&& f, std::integer_sequence<int, Is...>) { (f(IC<Is>{}), ...); }
template <int N, class F> DI void sfor(F&& f) { sfor_impl(f, std::make_integer_sequence<int, N>{}); }
DI int lane_id() { int l; asm volatile("v_mbcnt_lo_u32_b32 %0, -1, 0\n\tv_mbcnt_hi_u32_b32 %0, -1, %0" : "=v"(l)); return l; }

DI void set_flag(char* ws, unsigned off, unsigned tlo, unsigned thi) { gs8c(off, ws, u32x2{tlo, thi}); }
DI void poll_flags(const char* ws, unsigned off, bool need, unsigned tlo, unsigned thi) {
    for (unsigned spins = 0; spins < (1u << 20); ++spins) { u32x2 v; gl8c<0>(v, off, ws); asm volatile("s_waitcnt vmcnt(0)" : "+v"(v) :: "memory");
        if (__all(!need || (v[0] == tlo && v[1] == thi))) break; } }
DI void poll_flags2(const char* ws, unsigned o1, unsigned o2, unsigned tlo, unsigned thi) {
    for (unsigned spins = 0; spins < (1u << 20); ++spins) { u32x2 v, u; gl8c<0>(v, o1, ws); gl8c<0>(u, o2, ws); asm volatile("s_waitcnt vmcnt(0)" : "+v"(v), "+v"(u) :: "memory");
        if (__all(v[0] == tlo && v[1] == thi && u[0] == tlo && u[1] == thi)) break; } }
DI bool dense_ok(const u32x4& v, const u32x4& u, unsigned tlo, unsigned thi) { unsigned d = ((v[0] ^ tlo) | (v[1] ^ thi) | (v[2] ^ tlo) | (v[3] ^ thi)) | ((u[0] ^ tlo) | (u[1] ^ thi) | (u[2] ^ tlo) | (u[3] ^ thi)); PIN(d); return __all(d == 0u); }
DI void poll_dense(const char* ws, unsigned base, int lane, unsigned tlo, unsigned thi) {
    for (unsigned spins = 0; spins < (1u << 20); ++spins) { u32x4 v, u; gl16c<0>(v, base + lane * 16, ws); gl16c<1024>(u, base + lane * 16, ws); asm volatile("s_waitcnt vmcnt(0)" : "+v"(v), "+v"(u) :: "memory");
        if (dense_ok(v, u, tlo, thi)) break; __builtin_amdgcn_s_sleep(4); } }

HD constexpr int voff(int r, int h) { return r < 16 ? h * 256 + r * 16 : 512 + h * 64 + (r - 16) * 16; }

struct Retile { u32x4 in[8]; unsigned e[8]; };
template <int J> DI void rt_load(Retile& R, const char* p, int lo, int hi) { const char* a = p + ((J & 3) * lo + (J >> 2) * hi);
    asm volatile("global_load_dwordx4 %0, %1, off" : "=v"(R.in[J]) : "v"(a) : "memory"); }
template <int J> DI void rt_load2(Retile& R, const char* p, int lo, int hi) { const char* a = p + ((J & 3) * lo + (J >> 2) * hi);
    asm volatile("global_load_ushort %0, %1, off" : "=v"(R.e[J]) : "v"(a) : "memory"); }
DI void rt_wait(Retile& R) { asm volatile("s_waitcnt vmcnt(0)" : "+v"(R.in[0]), "+v"(R.in[1]), "+v"(R.in[2]), "+v"(R.in[3]), "+v"(R.in[4]), "+v"(R.in[5]), "+v"(R.in[6]), "+v"(R.in[7]) :: "memory"); }
template <int I> DI u32x4 rt_col(const Retile& R) { u32x4 o;
    UNR for (int k = 0; k < 4; ++k) o[k] = __builtin_amdgcn_perm(R.in[2 * k + 1][I >> 1], R.in[2 * k][I >> 1], (I & 1) ? 0x07060302u : 0x05040100u); return o; }
DI u32x4 rt_col9(Retile& R) { asm volatile("" : "+v"(R.e[0]), "+v"(R.e[1]), "+v"(R.e[2]), "+v"(R.e[3]), "+v"(R.e[4]), "+v"(R.e[5]), "+v"(R.e[6]), "+v"(R.e[7]));
    return u32x4{R.e[0] | R.e[1] << 16, R.e[2] | R.e[3] << 16, R.e[4] | R.e[5] << 16, R.e[6] | R.e[7] << 16}; }

DI unsigned wd_pos(int r, int g) { return (r & 3) * 256 + ((g ^ (r & 3)) << 6) + (r >> 2) * 16; }
struct RtTask { const char* rp; unsigned ro, reo, ra, rd, rd9; };
DI void rt_setup(RtTask& T, const char* wg, const char* wu, const char* wd, int gw) {
    const int lane = lane_id(), rr = lane & 15, gq = lane >> 4, i = lane & 7;
    if (gw < 1296) { const int span = gw / 36, ks = gw - span * 36, T8 = 8 * span + i, fs = (T8 * 7282) >> 16, t = T8 - fs * 9;
        T.rp = (ks & 1) ? wu : wg; T.ro = (32 * (ks >> 1) + 8 * gq) * 9216 + span * 256 + rr * 16; T.reo = 0; T.rd9 = 0; T.ra = lane * 16;
        T.rd = WS_WGU + (fs * 324 + (t >= 3 ? 36 * t + ks : (ks < 24 ? 3 * ks : 48 + ks) + (ks < 24 ? 1 : 12) * t)) * 1024;
    } else { const int id = gw < 1872 ? gw - 1296 : 0;
        const int u = id >> 2, s4 = (u * 1821) >> 16, ul = u - 36 * s4, TT = 9 * (8 * s4 + 2 * (ul & 3) + (gq >> 1)) + (ul >> 2);
        T.rp = wd; T.ro = (128 * (TT >> 3) + (TT & 7) + 32 * (gq & 1)) * 1152 + (id & 3) * 256 + rr * 16; T.reo = T.ro + 1024 - (id & 3) * 224 - rr * 14;
        const int d = ((rr >> 2) - s4 - 1) & 3, p = ul >> 1; const unsigned base = WS_WD + ((id & 3) * 144 + s4 * 36) * 9216 + (ul & 1) * 1024;
        T.ra = base + (d < 3 ? 7 * p + 2 * d : 126 + 2 * p) * 2048 + (gq << 6) + (rr & 3) * 16; T.rd = (i & 1) * 2048 + (i >> 1) * 256; T.rd9 = base + (7 * p + 6) * 2048 + wd_pos(rr, gq); }
    asm volatile("" : "+s"(T.rp)); PIN(T.ro); PIN(T.reo); PIN(T.ra); PIN(T.rd); PIN(T.rd9); }

constexpr int K1_XB = 528, K1_W = 2 * 72 * K1_XB, K1_BLK = 7168, K1_RS = K1_W + 12 * K1_BLK;

DI int wrap36(int v) { return v >= 36 ? v - 36 : v; }
template <int MODE, class HK> DI void p1_loop(HK&& hook, bool task, f32x16& c0, f32x16& c1, f32x4 (&rs)[8], u32x4 (&wf)[8], unsigned ra, unsigned xa, unsigned xb, unsigned wa, const unsigned (&wv)[7], const char* wq, int w, int rot) {
    u32x4 x0[2], x1[2];
    auto rw = [&](auto K) { constexpr int ks = K; const u32x2 w0 = ldstr(wa + (ks % 12) * K1_BLK), w1 = ldstr(wa + (ks % 12) * K1_BLK + 1792); wf[ks % 8] = u32x4{w0[0], w0[1], w1[0], w1[1]}; };
    auto rx = [&](auto K) { constexpr int ks = K; x0[ks & 1] = lds128(xa + ks * 2 * K1_XB); x1[ks & 1] = lds128(xb + ks * 2 * K1_XB); };
    if constexpr (MODE != 2) rx(IC<0>{});
    sfor<9>([&](auto P) { constexpr int p = P;
        if constexpr (p > 0 && p < 8) {
            if constexpr (MODE == 2) { if constexpr (p >= 2) { if constexpr (p < 7) waitv<28>(); else { if (task) waitv<17>(); else waitv<0>(); } } lbar(); }
            else { if constexpr (p == 1) { if (w < 4) waitv<0>(); } asm volatile("s_barrier" ::: "memory"); } }
        if constexpr (p == 8 && MODE == 1) { UNR for (int i = 0; i < 8; ++i) rs[i] = *(const LAS f32x4*)(ra + i * 32); }
        if constexpr (p == 8 && MODE == 0) { rs[0][0] = *(const LAS float*)(ra); rs[0][1] = *(const LAS float*)(ra + 128); }
        if constexpr (MODE == 2) { if constexpr (p <= 5) { const char* s = wq + wrap36(4 * (p + 3) + rot) * (16 * 3456);
            sfor<28>([&](auto I) { constexpr int b = I / 7, i = I % 7; glds16i<K1_W + ((4 * p + b) % 12) * K1_BLK + i * 1024>(wv[i], s + b * (16 * 3456)); }); } }
        else sfor<4>([&](auto KK) { constexpr int ks = 4 * p + KK;
            if constexpr (ks + 1 < 36) rx(IC<ks + 1>{});
            if constexpr (MODE) c0 = mfma32(x0[ks & 1], wf[ks % 8], c0); else c0 = mfma32(wf[ks % 8], x0[ks & 1], c0);
            SBAR(); hook(IC<2 * ks>{}); SBAR();
            if constexpr (p < 8 && KK < 2) { rw(IC<4 * p + 4 + 2 * KK>{}); rw(IC<4 * p + 5 + 2 * KK>{}); }
            if constexpr (MODE) c1 = mfma32(x1[ks & 1], wf[ks % 8], c1); else c1 = mfma32(wf[ks % 8], x1[ks & 1], c1);
            SBAR(); hook(IC<2 * ks + 1>{}); SBAR(); });
        if constexpr (MODE == 2) sfor<8>([&](auto KK) { hook(IC<8 * p + KK>{}); });
    });
}

template <class PF> DI void phase1(PF&& prefetch, const char* x, const char* wqkv, const char* wo, char* ws, int w, int mt, int ct) {
    const int lane = lane_id();
    u32x4 xv[9];
    { const unsigned vo = (unsigned)((w * 8 + (lane >> 3)) * 1152 + (lane & 7) * 16); const char* xs = x + (size_t)mt * 64 * 1152;
      sfor<9>([&](auto I) { const int g9 = I + ((mt & 3) * 2); gl16<0>(xv[I], vo, xs + (g9 >= 9 ? g9 - 9 : g9) * 128); }); }
    asm volatile("s_barrier" ::: "memory");
    prefetch(lane, false);
    const bool idle = w == 7; const int id = ct * 32 + mt; Retile R; u32x4 r9;
    unsigned wv[7];
    UNR for (int i = 0; i < 7; ++i) { const int c = i * 64 + lane, row = (c * 2341) >> 16, ch = c - row * 28; wv[i] = (unsigned)(row * 3456 + (ch < 27 ? ch : 26) * 16); }
    const int rot = ((mt & 3) * 2) * 4; const char* wq = wqkv + ct * 432; const char* wq0 = wq + wrap36(w + rot) * (16 * 3456); const char* wq1 = wq + wrap36(w + 8 + rot) * (16 * 3456);
    auto dm = [&](auto I) { constexpr int i = I; SBAR();
        if constexpr (i < 7) glds16(wv[i], wq0, K1_W + w * K1_BLK + i * 1024);
        else { if (w < 4) glds16(wv[i - 7], wq1, K1_W + (w + 8) * K1_BLK + (i - 7) * 1024); } SBAR(); };
    auto dm1 = [&](auto I) { constexpr int i = I; if (w >= 4) { if constexpr (i < 5) dm(IC<2 + i>{}); } else dm(IC<7 + i>{}); };
    const int r = lane & 31, hh = lane >> 5;
    unsigned xa = hh * K1_XB + r * 16, xb = xa + 72 * K1_XB, wa;
    { const int cg = (lane >> 4) & 1, q = (lane & 15) >> 2, p = lane & 3;
      wa = K1_W + (8 * hh + q) * 448 + (w < 6 ? 72 * w + cg * 40 + 8 * p : 32 + 72 * (cg ? 4 + (p & 1) : p)); }
    const int xrow = w * 8 + (lane >> 3); unsigned xw = ((xrow >> 5) * 72 + (lane & 7)) * K1_XB + (xrow & 31) * 16;
    if (w < 4) sfor<7>(dm); else sfor<2>(dm);
    PIN(xa); PIN(xb); PIN(wa); PIN(xw);
    if (w < 4) waitv<7>(); else waitv<2>();
    asm volatile("" : "+v"(xv[0]), "+v"(xv[1]), "+v"(xv[2]), "+v"(xv[3]), "+v"(xv[4]), "+v"(xv[5]), "+v"(xv[6]), "+v"(xv[7]), "+v"(xv[8]));
    float mean; u32x4 wf[8];
    { float s0 = 0, s1 = 0, s2 = 0, s3 = 0; unsigned onev = 0x3f803f80u; PIN(onev);
      sfor<9>([&](auto I) { constexpr int i = I; s0 = dot2(xv[i][0], onev, s0); s1 = dot2(xv[i][1], onev, s1); s2 = dot2(xv[i][2], onev, s2); s3 = dot2(xv[i][3], onev, s3);
          if constexpr (i == 4) { PIN(s0); dm1(IC<0>{}); } });
      mean = red8((s0 + s1) + (s2 + s3)) * (1.f / 576.f); }
    { unsigned slo = 0x00003f80u, shi = 0x3f800000u; float nm = -mean; PIN(slo); PIN(shi); PIN(nm);
      sfor<9>([&](auto I) { constexpr int i = I; xv[i] = addc8(xv[i], slo, shi, nm); sts128(xw + i * 8 * K1_XB, xv[i]);
          if constexpr (i < 4) { if (w >= 4) dm1(IC<1 + i>{}); }
          if constexpr (i == 5) { if (w < 4) waitv<1>(); asm volatile("s_barrier" ::: "memory");
              UNR for (int k = 0; k < 4; ++k) { const u32x2 w0 = ldstr(wa + k * K1_BLK), w1 = ldstr(wa + k * K1_BLK + 1792); wf[k] = u32x4{w0[0], w0[1], w1[0], w1[1]}; } }
          if constexpr (i >= 6) { if (w < 4) { dm1(IC<2 * i - 11>{}); dm1(IC<2 * i - 10>{}); } } }); }
    if (w >= 4) waitv<0>();
    lbar();
    const char* tp; int tlo_, thi_, teo;
    auto taskld = [&](auto H) { constexpr int h = H; const bool fr = id < 72;
        if constexpr (h == 0) { const int j = id & 3, u = (fr ? id : id - 72) >> 2, rr = lane & 15, gq = fr ? lane >> 4 : j, oc = 4 * (rr & 3) + (rr >> 2), jr = fr ? 0 : lane >> 3;
            const int row = u < 16 ? 36 * u + (gq >> 1) * 20 + 4 * (gq & 1) + (jr & 3) + (jr >> 2) * 8 : 36 * (8 * (u - 16) + 2 * gq + (jr >> 2)) + 16 + (jr & 3);
            tp = wo + (row * 1152 + (fr ? j * 288 + oc * 16 : (lane & 7) * 16)); tlo_ = fr ? 1152 : 128; thi_ = fr ? (u < 16 ? 9216 : 41472) : 512; teo = fr ? 256 + rr * 2 - oc * 16 : 0;
            asm volatile("global_load_dwordx4 %0, %1, off" : "=v"(r9) : "v"(tp + (fr ? 0 : 1024)) : "memory"); sfor<2>([&](auto J) { rt_load<J>(R, tp, tlo_, thi_); rt_load2<J>(R, tp + teo, tlo_, thi_); }); }
        else sfor<6>([&](auto J) { rt_load<2 + J>(R, tp, tlo_, thi_); rt_load2<2 + J>(R, tp + teo, tlo_, thi_); }); };
    unsigned vst; float q0 = 0.f, q1 = 0.f;
    auto hk = [&](auto K) { constexpr int k = K;
        if constexpr (k < 36) { const unsigned u = xv[k >> 2][k & 3]; const float a = bflo(u), b = bfhi(u); q0 = fmaf(a, a, q0); q1 = fmaf(b, b, q1); PIN(q0); PIN(q1); }
        if constexpr (k == 36) { q0 = red8(q0 + q1); PIN(q0); }
        if constexpr (k == 37) { const float rstd = __builtin_amdgcn_rsqf(q0 * (1.f / 576.f) + 1e-5f); *(LAS float*)(K1_RS + xrow * 4) = rstd; }
        if constexpr (k == 57) prefetch(lane, true);
        if constexpr (k == 44) {
            vst = K1_W + hh * 512 + (r >> 4) * 64 + ((r >> 2) & 1) * 32 + ((r >> 3) & 1) * 8 + (r & 3) * 2; PIN(vst); } };
    auto hk2 = [&](auto K) { if constexpr (K == 48 || K == 49) { if (id < 144) taskld(IC<K - 48>{}); } hk(K); };
    f32x16 c0 = {}, c1 = {}; f32x4 rsv[8];
    const int gp = 6 * ct + w;
    const bool vor = w < 6 && gp >= 32;
    if (idle) {
        p1_loop<2>(hk2, id < 144, c0, c1, rsv, wf, 0, xa, xb, wa, wv, wq, w, rot);
        if (id < 144) { rt_wait(R); PIN(r9);
            if (id < 72) { char* d = ws + WS_WO + ((id & 3) * 18 + (id >> 2)) * 9216;
                sfor<8>([&](auto I) { gs16c(lane * 16, d + I * 1024, rt_col<I>(R)); }); gs16c(lane * 16, d + 8192, rt_col9(R));
            } else { float s = 0.f; UNR for (int c = 0; c < 4; ++c) { s += bflo(r9[c]) + bfhi(r9[c]); UNR for (int i = 0; i < 8; ++i) s += bflo(R.in[i][c]) + bfhi(R.in[i][c]); }
                s = red8(s) * (1.f / 576.f);
                const unsigned hb = cvtpk(s, s) & 0xffffu; const float rest = s - bflo(hb); const unsigned both = hb | (cvtpk(rest, rest) & 0xffff0000u); u32x4 o;
                UNR for (int c = 0; c < 4; ++c) { const unsigned a = __builtin_amdgcn_readlane(both, 16 * c), b = __builtin_amdgcn_readlane(both, 16 * c + 8);
                    const unsigned v0 = (a & 0xffffu) | b << 16, v1 = (a >> 16) | (b & 0xffff0000u); o[c] = lane == 0 ? v0 : lane == 1 ? v1 : 0u; }
                if (lane < 16) gs16c(WS_WBAR + (id - 72) * 256 + lane * 16, ws, o); } }
    } else if (vor) p1_loop<1>(hk, false, c0, c1, rsv, wf, K1_RS + hh * 16, xa, xb, wa, wv, wq, w, rot); else p1_loop<0>(hk, false, c0, c1, rsv, wf, K1_RS + r * 4, xa, xb, wa, wv, wq, w, rot);
    constexpr float QS = 0.23570226039551584f * 1.4426950408889634f;
#define PK8(c, o, f) u32x4{cvtpk(c[o] * f, c[o + 1] * f), cvtpk(c[o + 2] * f, c[o + 3] * f), cvtpk(c[o + 4] * f, c[o + 5] * f), cvtpk(c[o + 6] * f, c[o + 7] * f)}
    if (!idle) {
        if (!vor) { const float r0 = rsv[0][0], r1 = rsv[0][1];
            if (w < 6) { const bool isq = gp < 16; const float f = isq ? QS : 1.f, f0 = f * r0, f1 = f * r1; const unsigned hs = isq ? 32 * QIMG : 32 * KVIMG;
                char* d = ws + (isq ? WS_Q + (gp * 64 + mt) * QIMG : WS_KV + ((gp - 16) * 64 + mt) * KVIMG);
                gs16c(lane * 16, d, PK8(c0, 0, f0)); gs16c(lane * 16, d + 1024, PK8(c1, 0, f1)); gs16c(lane * 16, d + hs, PK8(c0, 8, f0)); gs16c(lane * 16, d + hs + 1024, PK8(c1, 8, f1));
            } else { UNR for (int i = 0; i < 3; ++i) { const int g0 = 6 * ct + 2 * i; const bool isq = g0 < 16; const float f = isq ? QS : 1.f, f0 = f * r0, f1 = f * r1;
                    { UNR for (int e = 0; e < 2; ++e) {
                        const unsigned v0 = cvtpk(c0[4 * i + 2 * e] * f0, c0[4 * i + 2 * e + 1] * f0), v1 = cvtpk(c1[4 * i + 2 * e] * f1, c1[4 * i + 2 * e + 1] * f1);
                        if (isq) { char* d = ws + WS_Q + ((2 * g0 + e) * 32 + mt) * QIMG + 2048; gs4c(hh * 64 * QIMG + r * 4, d, v0); gs4c(hh * 64 * QIMG + r * 4, d + 128, v1); }
                        else if (g0 < 32) gs8c(hh * 64 * KVIMG + r * 8, ws + WS_KV + ((2 * (g0 - 16) + e) * 32 + mt) * KVIMG + 2048, u32x2{v0, v1});
                        else { const unsigned a = vst + (4 * i + e) * 256;
                            *(LAS unsigned short*)(a) = (unsigned short)v0; *(LAS unsigned short*)(a + 16) = (unsigned short)(v0 >> 16);
                            *(LAS unsigned short*)(a + 128) = (unsigned short)v1; *(LAS unsigned short*)(a + 144) = (unsigned short)(v1 >> 16); } } } }
                if (ct >= 5) { u32x4 cv[6]; const int sl = lane & 3; const unsigned fill = sl == 2 ? 0x3f803f80u : 0u;
                    UNR for (int i = 0; i < 6; ++i) cv[i] = lds128(K1_W + i * 512 + (lane >> 2) * 32 + (lane & 1) * 16);
                    UNR for (int i = 0; i < 6; ++i) if (6 * ct + i >= 32)
                        gs16c(hh * 32 * KVIMG + ((lane >> 3) & 3) * 640 + (lane & 7) * 16, ws + WS_KV + ((6 * ct + i - 32) * 64 + mt) * KVIMG + 2304 + 512,
                              u32x4{sl < 2 ? cv[i][0] : fill, sl < 2 ? cv[i][1] : fill, sl < 2 ? cv[i][2] : fill, sl < 2 ? cv[i][3] : fill}); } }
        } else {
            UNR for (int i = 0; i < 4; ++i) UNR for (int e = 0; e < 4; ++e) { c0[4 * i + e] *= rsv[i][e]; c1[4 * i + e] *= rsv[4 + i][e]; }
            char* d = ws + WS_KV + ((gp - 32) * 64 + mt) * KVIMG + 2304; const unsigned o = (r >> 4) * 32 * KVIMG + hh * 256 + (r & 15) * 16;
            gs16c(o, d, PK8(c0, 0, 1.f)); gs16c(o, d + 640, PK8(c0, 8, 1.f)); gs16c(o, d + 1280, PK8(c1, 0, 1.f)); gs16c(o, d + 1920, PK8(c1, 8, 1.f)); } }
#undef PK8
    if (ct == 0 && (lane & 7) == 0) gs4c(WS_XMEAN + (mt * 64 + xrow) * 4, ws, __float_as_uint(mean));
}

constexpr int K2_IMG = 32 * KVIMG, K2_HALF = 16 * KVIMG;
struct P3Pre { u32x4 wf[9][3], xa, xb; unsigned xm; };

DI float xhalf_max(float m) { auto rr = __builtin_amdgcn_permlane32_swap(__float_as_uint(m), __float_as_uint(m), false, false);
    return __builtin_fmaxf(__uint_as_float(rr[0]), __uint_as_float(rr[1])); }
DI float max16(const f32x16& s) { float a = __builtin_fmaxf(__builtin_fmaxf(s[0], s[1]), s[2]), b = __builtin_fmaxf(__builtin_fmaxf(s[3], s[4]), s[5]);
    a = __builtin_fmaxf(__builtin_fmaxf(a, s[6]), s[7]); b = __builtin_fmaxf(__builtin_fmaxf(b, s[8]), s[9]); a = __builtin_fmaxf(__builtin_fmaxf(a, s[10]), s[11]);
    b = __builtin_fmaxf(__builtin_fmaxf(b, s[12]), s[13]); a = __builtin_fmaxf(__builtin_fmaxf(a, s[14]), s[15]); return __builtin_fmaxf(a, b); }

template <class PF> DI void phase2(PF&& prefetch2, const char* x, const RtTask& T, char* ws, P3Pre& P, unsigned po1, unsigned po2, int w, int head, int qb, int t3, int j3, unsigned tlo, unsigned thi) {
    const int lane = lane_id(), r = lane & 31, hh = lane >> 5, kh = w >> 2, gw = blockIdx.x * 8 + w;
    const unsigned lv = lane * 16, lk = kh * K2_HALF + (w & 3) * 1024;
    u32x4 qa, qb4; unsigned qra, qrb;
    const char* qs = ws + WS_Q + (head * 32 + qb * 4 + (w & 3)) * QIMG; const char* kv = ws + WS_KV + head * K2_IMG + lk;
    unsigned ka = kh * K2_HALF + lv, kra = kh * K2_HALF + (hh ? 2304 + 544 : 2048 + r * 8), va = kh * K2_HALF + 2304 + voff(r < 19 ? r : r == 22 ? 18 : 19, hh);
    unsigned q0 = lv + kh * 1024, q1 = lv + 1024 - kh * 1024, q2 = r * 4 + kh * 128, q3 = r * 4 + 128 - kh * 128;
    PIN(ka); PIN(kra); PIN(va); PIN(q0); PIN(q1); PIN(q2); PIN(q3); asm volatile("" : "+s"(qs), "+s"(kv));
    if (w == 0 && lane == 0) *(LAS unsigned*)(K2_IMG + 7936) = 0u;
    if (w == 1) poll_flags2(ws, po1, po2, tlo, thi);
    asm volatile("s_barrier" ::: "memory");
    UNR for (int j = 0; j < 5; ++j) glds16c(lv, kv + j * 4096, lk + j * 4096);
    gl16c<0>(qa, q0, qs); gl16c<0>(qb4, q1, qs); gl4c<2048>(qra, q2, qs); gl4c<2048>(qrb, q3, qs);
    Retile R; const bool isgu = gw < 1296, isd = !isgu && gw < 1872; const int rhi = isgu ? 36864 : 73728;
    auto rtl = [&](auto J) { gl16<0>(R.in[J], T.ro, T.rp + ((J & 3) * 9216 + (J >> 2) * rhi)); };
    auto rtl2 = [&](auto J) { if (isd) gl2(R.e[J], T.reo, T.rp + ((J & 3) * 9216 + (J >> 2) * 73728)); };
    if (w >= 4) __builtin_amdgcn_s_setprio(1);
    asm volatile("s_waitcnt vmcnt(0) lgkmcnt(0)\n\ts_barrier" : "+v"(qa), "+v"(qb4), "+v"(qra), "+v"(qrb) :: "memory");
    const f32x16 z = {};
    f32x16 oa = {}, ob = {}, sa, sb; u32x4 p0, p1 = {0u, 0u, 0u, 0u}, kf = lds128(ka), vf0, vf1 = {0u, 0u, 0u, 0u}; u32x2 kr = lds64(kra), va0, va1, vb0, vb1; float ma, mb;
    { const u32x2 ta = {hh ? 0u : qra, 0u}, tb = {hh ? 0u : qrb, 0u};
      sa = mfma32(kf, qa, z); sb = mfma32(kf, qb4, z); sa = mfma32k8(kr, ta, sa); sb = mfma32k8(kr, tb, sb);
      UNR for (int j = 5; j < 7; ++j) glds16c(lv, kv + j * 4096, lk + j * 4096);
      const unsigned mp = cvtpk(-xhalf_max(max16(sa)), -xhalf_max(max16(sb)));
      ma = -bflo(mp); mb = -bfhi(mp);
      va0 = u32x2{hh ? mp & 0xffffu : qra, 0u}; va1 = u32x2{hh ? mp & 0xffffu : 0u, hh ? 0u : qra}; vb0 = u32x2{hh ? mp >> 16 : qrb, 0u}; vb1 = u32x2{hh ? mp >> 16 : 0u, hh ? 0u : qrb};
      UNR for (int i = 0; i < 16; ++i) sa[i] -= ma; }
#define EX(v) __builtin_amdgcn_exp2f(v)
#define QUAD(S, i, P, j) do { const float e0 = EX(S[i]), e1 = EX(S[i + 1]), e2 = EX(S[i + 2]), e3 = EX(S[i + 3]); P[j] = cvtpk(e0, e1); P[j + 1] = cvtpk(e2, e3); PIN(P); SBAR(); } while (0)
#define STEP(SC, SN, QN, VN, OP, OC, LD, KOFF, VOFF, LDKR, H, H0) do { \
        if (LD) { vf0 = lds128(va + (VOFF)); SBAR(); } \
        SN = mfma32k8(kr, VN, z); SBAR(); if (LDKR) kr = lds64(kra + (KOFF)); QUAD(SC, 0, p0, 0); \
        SN = mfma32(kf, QN, SN); SBAR(); if (LD) kf = lds128(ka + (KOFF)); H(IC<H0>{}); QUAD(SC, 4, p0, 2); \
        OP = mfma32(vf1, p1, OP); SBAR(); if (LD) vf1 = lds128(va + (VOFF) + 640); H(IC<H0 + 1>{}); QUAD(SC, 8, p1, 0); \
        OC = mfma32(vf0, p0, OC); SBAR(); H(IC<H0 + 2>{}); QUAD(SC, 12, p1, 2); } while (0)
#define GROUP(G, H) do { constexpr int B_ = ((G) & 7) * KVIMG; \
        STEP(sa, sb, qb4, vb0, ob, oa, true, B_ + 1024, B_, false, H, 0); STEP(sb, sa, qa, va1, oa, ob, false, 0, 0, false, H, 3); \
        STEP(sa, sb, qb4, vb1, ob, oa, true, B_ + KVIMG, B_ + 1280, true, H, 6); STEP(sb, sa, qa, va0, oa, ob, false, 0, 0, false, H, 9); \
        if constexpr ((G) == 7) { ka += 8 * KVIMG; va += 8 * KVIMG; kra += 8 * KVIMG; PIN(ka); PIN(va); PIN(kra); } } while (0)
    auto none = [&](auto) {};
    u32x4 fd0, fd1;
    { auto h = [&](auto I) { if constexpr (I % 2 == 0) glds16c(lv, kv + (7 + I / 2) * 4096, lk + (7 + I / 2) * 4096); }; GROUP(0, h); }
    { auto h = [&](auto I) { if constexpr (I % 2 == 0) glds16c(lv, kv + (13 + I / 2) * 4096, lk + (13 + I / 2) * 4096);
                            if constexpr (I == 11) { if (w == 4) { gl16c<0>(fd0, WS_FD + lv, ws); gl16c<1024>(fd1, WS_FD + lv, ws); } } }; GROUP(1, h); }
    __builtin_amdgcn_s_setprio(0);
    { auto h = [&](auto I) { if constexpr (I % 3 == 0) rtl(IC<I / 3>{}); if constexpr (I % 3 == 1) rtl2(IC<I / 3>{}); }; GROUP(2, h); }
    if (isd) waitv<8>(); else waitv<4>();
    if (w == 4) { asm volatile("" : "+v"(fd0), "+v"(fd1));
        if (!dense_ok(fd0, fd1, tlo, thi)) poll_dense(ws, WS_FD, lane, tlo, thi); }
    asm volatile("s_barrier" ::: "memory");
    if (w >= 4) __builtin_amdgcn_s_setprio(1);
    { auto h = [&](auto I) { if constexpr (I % 3 == 0) rtl(IC<4 + I / 3>{}); if constexpr (I % 3 == 1) rtl2(IC<4 + I / 3>{}); }; GROUP(3, h); }
    GROUP(4, none); GROUP(5, none); GROUP(6, none); GROUP(7, none);
    rt_wait(R);
    unsigned xm6 = isd ? 64u : 0u; asm volatile("" : "+s"(xm6));
    auto rts = [&](auto C) { constexpr int c = C; if (gw < 1872) gs16c((T.ra ^ (xm6 * (c >> 1))) + __builtin_amdgcn_readlane(T.rd, c), ws, rt_col<c>(R)); };
    { auto h = [&](auto I) { if constexpr (I % 3 == 0) rts(IC<I / 3>{}); }; GROUP(8, h); }
    { auto h = [&](auto I) { if constexpr (I % 3 == 0) rts(IC<4 + I / 3>{}); if constexpr (I == 11) { if (isd) gs16c(T.rd9, ws, rt_col9(R)); } }; GROUP(9, h); }
    { const int ng = (w & 3) ^ (kh * 2), tid = w * 64 + lane;
      const char* wsrc = ws + WS_WO + ((j3 * 18 + 9 * kh) * 9 + ng * 2) * 1024; const char* w3 = ng ? ws + WS_WBAR + 9 * kh * 1024 : wsrc + 8192; const int s3 = ng ? 1024 : 9216;
      auto ld = [&](auto N) { constexpr int n = N;
          if constexpr (n < 18) gl16c<(n & 1) * 1024>(P.wf[n / 2][n & 1], lv, wsrc + n / 2 * 9216);
          if constexpr (n == 18) gl16<0>(P.xa, r * 1152 + (tid >> 5) * 16, x + (size_t)t3 * 32 * 1152 + j3 * 288);
          if constexpr (n == 19) gl16<256>(P.xb, r * 1152 + hh * 16, x + (size_t)t3 * 32 * 1152 + j3 * 288);
          if constexpr (n == 20) gl4c<0>(P.xm, r * 4, ws + WS_XMEAN + t3 * 128); };
      { auto h = [&](auto I) { ld(I); }; GROUP(10, h); }
      { auto h = [&](auto I) { ld(IC<12 + I>{}); if constexpr (I == 10) __builtin_amdgcn_s_setprio(0); }; GROUP(11, h); }
      { waitv<21>(); unsigned old = 0; if (lane == 0) old = __hip_atomic_fetch_add((LAS unsigned*)(K2_IMG + 7936), 1u, __ATOMIC_RELAXED, __HIP_MEMORY_SCOPE_WORKGROUP);
        if (lane == 0 && old == 7u) set_flag(ws, WS_FR + blockIdx.x * 8, tlo, thi); }
      { auto h = [&](auto I) { if constexpr (I < 9) { if (ng < 2) gl16c<0>(P.wf[I][2], lv, w3 + I * s3); } }; GROUP(12, h); } }
    prefetch2(lane);
    auto box = [&](int v) { return (unsigned)((v & 3) < 3 ? (v >> 2) * K2_HALF + (v & 3) * 3072 : K2_IMG + 1024 + (v >> 2) * 3072) + lv; };
    const int tt = qb * 8 + (w & 3) * 2 + kh;
    unsigned bx0 = box(w), bx1 = box(w ^ 4), oadr = WS_O + ((tt * 2 + (r >> 4)) * 16 + (head >> 1)) * 1024 + (2 * (head & 1) + hh) * 256 + (r & 15) * 16, radr = WS_OR + (tt * 32 + head) * 128 + r * 4;
    PIN(bx0); PIN(bx1); PIN(oadr); PIN(radr);
    GROUP(13, none); GROUP(14, none); GROUP(15, none);
    ob = mfma32(vf1, p1, ob);
    { float n0 = 0.f, n1 = 0.f, n2 = 0.f, n3 = 0.f;
      UNR for (int i = 0; i < 10; i += 2) { n0 = fmaf(oa[i], 0.f, n0); n1 = fmaf(oa[i + 1], 0.f, n1); n2 = fmaf(ob[i], 0.f, n2); n3 = fmaf(ob[i + 1], 0.f, n3); }
      const float lo = __builtin_fminf(oa[10], ob[10]) + ((n0 + n1) + (n2 + n3)), hi = oa[10] + ob[10];
      const bool bad = !(lo > 1e-6f && hi < 1e12f);
      if (__builtin_expect(__any(bad), 0)) { ka -= 8 * KVIMG; va -= 8 * KVIMG; kra -= 8 * KVIMG; const char* img = ws + WS_KV + head * K2_IMG;
        auto sc = [&](int t, const u32x4& q, const u32x2& t0, const u32x2& t1) { const unsigned cb = (t >> 1) * KVIMG; u32x4 k1; u32x2 k2; gl16c<0>(k1, ka + cb + (t & 1) * 1024, img); gl8c<0>(k2, kra + cb, img);
            asm volatile("s_waitcnt vmcnt(0)" : "+v"(k1), "+v"(k2) :: "memory"); return mfma32k8(k2, (t & 1) ? t1 : t0, mfma32(k1, q, z)); };
        auto redo = [&](const u32x4& q, unsigned q2, f32x16& o, float& m) { const u32x2 t0 = {q2, 0u}, t1 = {0u, q2}; m = -__builtin_inff();
            for (int t = 0; t < 32; ++t) m = __builtin_fmaxf(m, max16(sc(t, q, t0, t1)));
            m = xhalf_max(m); o = z;
            for (int t = 0; t < 32; ++t) { const unsigned cb = (t >> 1) * KVIMG + (t & 1) * 1280; u32x4 v0, v1, a, b; gl16c<0>(v0, va + cb, img); gl16c<640>(v1, va + cb, img);
                const f32x16 s = sc(t, q, t0, t1); asm volatile("" : "+v"(v0), "+v"(v1));
                UNR for (int i = 0; i < 4; ++i) { a[i] = cvtpk(EX(s[2 * i] - m), EX(s[2 * i + 1] - m)); b[i] = cvtpk(EX(s[8 + 2 * i] - m), EX(s[9 + 2 * i] - m)); }
                o = mfma32(v0, a, o); o = mfma32(v1, b, o); } };
        redo(qa, hh ? 0u : va0[0], oa, ma); redo(qb4, hh ? 0u : vb0[0], ob, mb); } }
    ob[11] = mb;
    UNR for (int i = 0; i < 3; ++i) *(LAS f32x4*)(bx0 + i * 1024) = f32x4{ob[4 * i], ob[4 * i + 1], ob[4 * i + 2], ob[4 * i + 3]};
    lbar();
    { f32x4 rv[3]; UNR for (int i = 0; i < 3; ++i) rv[i] = *(const LAS f32x4*)(bx1 + i * 1024);
      const float M = __builtin_fmaxf(ma, rv[2][3]); float f0 = EX(ma - M), f1 = EX(rv[2][3] - M), o[10];
      const float il = __builtin_amdgcn_rcpf(fmaf(oa[10], f0, rv[2][2] * f1)); f0 *= il; f1 *= il;
      UNR for (int i = 0; i < 10; ++i) { o[i] = fmaf(oa[i], f0, rv[i >> 2][i & 3] * f1); PIN(o[i]); }
      gs16c(oadr, ws, u32x4{cvtpk(o[0], o[1]), cvtpk(o[2], o[3]), cvtpk(o[4], o[5]), cvtpk(o[6], o[7])});
      if (hh == 0) gs4c(radr, ws, cvtpk(o[8], o[9]));
      u32x4 fr0, fr1; if (w == 0) { gl16c<0>(fr0, WS_FR + lv, ws); gl16c<1024>(fr1, WS_FR + lv, ws); }
      waitv<0>(); if (lane == 0) set_flag(ws, radr + (WS_F2 - WS_OR), tlo, thi);
      if (w == 0) { asm volatile("" : "+v"(fr0), "+v"(fr1)); if (!dense_ok(fr0, fr1, tlo, thi)) poll_dense(ws, WS_FR, lane, tlo, thi); } }
#undef STEP
#undef GROUP
#undef QUAD
}

struct WStream { const char* a; const char* b; unsigned lv, lw; };
template <int J> DI void wround(const WStream& S, int w) {
    const char* p = J < 40 ? S.a + J * 8192 : J > 40 ? S.b + (J - 40) * 8192 : (w < 4 ? S.a + J * 8192 : S.b);
    glds16c(S.lv, p, S.lw + (J % 18) * 8192); }

constexpr int K3_ROW = 164 * 4, K3_L = LDS_BYTES - 2 * 32 * K3_ROW, K3_O = 84992;
DI void phase3(char* ws, P3Pre& P, const WStream& S, int w, int tt, int j, int qb, unsigned tlo, unsigned thi) {
    const int lane = lane_id(), kg = w >> 2, ng = (w & 3) ^ (kg * 2);
    const unsigned lv = lane * 16;
    f32x4 acc[3][2] = {};
    const char* os = ws + WS_O + tt * 32768 + w * 4096; const char* rs = ws + WS_OR + tt * 4096 + (w & 3) * 1024;
    unsigned oa = K3_O + kg * 9216 + lv, ro = K3_O + 32768 + (lane >> 4) * 512 + (lane & 15) * 4;
    unsigned wb = K3_L + (kg * 32 + (lane & 15)) * K3_ROW + (lane >> 4) * 32 + ng * 8, w9 = K3_L + (kg * 32 + (lane & 15)) * K3_ROW + 512 + ng * 64 + (lane >> 4) * 16;
    unsigned rb = K3_L + (lane & 31) * K3_ROW, ra = rb + (2 * w + (lane >> 5)) * 32, dst = WS_HC + (tt * 2 + ((lane >> 4) & 1)) * 18432 + (18 * j + (lane >> 5)) * 256 + (lane & 15) * 16;
    PIN(wb); PIN(w9); PIN(rb); PIN(ra); PIN(dst); PIN(ro); PIN(oa); asm volatile("" : "+s"(rs), "+s"(os));
    f32x4 xa0 = {bflo(P.xa[0]), bfhi(P.xa[0]), bflo(P.xa[1]), bfhi(P.xa[1])}, xa1 = {bflo(P.xa[2]), bfhi(P.xa[2]), bflo(P.xa[3]), bfhi(P.xa[3])};
    f32x4 xb0 = {bflo(P.xb[0]), bfhi(P.xb[0]), bflo(P.xb[1]), bfhi(P.xb[1])}, xb1 = {bflo(P.xb[2]), bfhi(P.xb[2]), bflo(P.xb[3]), bfhi(P.xb[3])};
    PIN(xa0); PIN(xa1); PIN(xb0); PIN(xb1);
    if (w == 2) poll_flags(ws, WS_F2 + (tt * 32 + (lane & 31)) * 128, true, tlo, thi);
    else sfor<10>([&](auto J) { wround<J>(S, w); });
    asm volatile("s_barrier" ::: "memory");
    UNR for (int i = 0; i < 4; ++i) glds16c(lv, os + i * 1024, K3_O + w * 4096 + i * 1024);
    if (w >= 4) glds16c(lv, rs, K3_O + 32768 + (w & 3) * 1024);
    waitv_bar<0>();
    u32x4 of[9][2];
    UNR for (int i = 0; i < 7; ++i) { of[i][0] = lds128(oa + i * 1024); of[i][1] = lds128(oa + i * 1024 + 16384); }
    if (kg == 0) { UNR for (int i = 7; i < 9; ++i) { of[i][0] = lds128(oa + i * 1024); of[i][1] = lds128(oa + i * 1024 + 16384); } }
    else { UNR for (int i = 0; i < 16; ++i) of[7 + (i >> 3)][(i >> 2) & 1][i & 3] = *(const LAS unsigned*)(ro + (i >> 3) * 2048 + ((i >> 2) & 1) * 64 + (i & 3) * 128); }
    UNR for (int i = 0; i < 9; ++i) UNR for (int c = 0; c < 2; ++c) { acc[c][0] = mfma16(P.wf[i][c], of[i][0], acc[c][0]); acc[c][1] = mfma16(P.wf[i][c], of[i][1], acc[c][1]); }
    if (ng < 2) { UNR for (int i = 0; i < 9; ++i) { acc[2][0] = mfma16(P.wf[i][2], of[i][0], acc[2][0]); acc[2][1] = mfma16(P.wf[i][2], of[i][1], acc[2][1]); } }
    UNR for (int b = 0; b < 2; ++b) {
        UNR for (int e = 0; e < 4; ++e) *(LAS f32x2*)(wb + b * 16 * K3_ROW + e * 128) = f32x2{acc[0][b][e], acc[1][b][e]};
        if (ng < 2) *(LAS f32x4*)(w9 + b * 16 * K3_ROW) = acc[2][b]; }
    if (w == 2) sfor<10>([&](auto J) { wround<J>(S, w); });
    lbar();
    f32x2 m2[2]; f32x4 pa[2], pb[2], pc[2], pd[2];
    UNR for (int k = 0; k < 2; ++k) { m2[k] = *(const LAS f32x2*)(rb + k * 32 * K3_ROW + 576); pa[k] = *(const LAS f32x4*)(ra + k * 32 * K3_ROW); pb[k] = *(const LAS f32x4*)(ra + k * 32 * K3_ROW + 16); }
    if (w == 7) { UNR for (int k = 0; k < 2; ++k) { pc[k] = *(const LAS f32x4*)(ra + k * 32 * K3_ROW + 64); pd[k] = *(const LAS f32x4*)(ra + k * 32 * K3_ROW + 80); } }
    SBAR();
    const f32x2 ms = m2[0] + m2[1]; const float cm = __uint_as_float(P.xm) + (ms[0] + ms[1]); float sq = 0.f, sq1 = 0.f;
    auto unit = [&](const f32x4 (&u)[2], const f32x4 (&v)[2], const f32x4& x0, const f32x4& x1, unsigned d) {
        const f32x4 a = (u[0] + u[1]) + (x0 - cm), b = (v[0] + v[1]) + (x1 - cm);
        gs16c(d, ws, u32x4{cvtpk(a[0], a[1]), cvtpk(a[2], a[3]), cvtpk(b[0], b[1]), cvtpk(b[2], b[3])});
        UNR for (int e = 0; e < 4; ++e) { sq = fmaf(a[e], a[e], sq); sq1 = fmaf(b[e], b[e], sq1); } };
    unit(pa, pb, xa0, xa1, dst + w * 512);
    if (w == 7) unit(pc, pd, xb0, xb1, dst + 4096);
    *(LAS float*)(81920 + (lane & 31) * 80 + (2 * w + (lane >> 5)) * 4) = sq + sq1;
    if (w == 6 && j == 0 && lane < 32) gs4c(WS_CM + tt * 128 + lane * 4, ws, __float_as_uint(cm));
    lbar();
    if (w == 5 && lane < 32) { f32x4 t4[4]; UNR for (int i = 0; i < 4; ++i) t4[i] = *(const LAS f32x4*)(81920 + lane * 80 + i * 16);
        const f32x4 t2 = (t4[0] + t4[1]) + (t4[2] + t4[3]); gs4c(WS_SSQ + (tt * 4 + j) * 128 + lane * 4, ws, __float_as_uint((t2[0] + t2[1]) + (t2[2] + t2[3]))); }
}

constexpr int FPF = 5, K4_X0 = 6, K5_AW = 6, NSLOT = 486, FW0 = 14;
HD constexpr int k4_tile(int q) { return q < 72 ? q % 3 : q < 108 ? (q - 72) / 12 : q / 36; }
HD constexpr int k4_ks(int q) { return q < 72 ? q / 3 : q < 108 ? 24 + (q - 72) % 12 : q % 36; }
HD constexpr bool f_bar(int g) { return g < 324 ? (g > 0 && g % 24 == 0) : g <= 432 ? (g - 324) % 18 == 0 : g == 443 || g == 453; }
HD constexpr int k5_per(int q) { return q < 126 ? q / 7 : (q - 126) / 2; }
HD constexpr int k5_tile(int q) { return q < 126 ? q % 7 : 7 + (q & 1); }
constexpr int NST = 4, ST_TILE[NST] = {3, 5, 7, 8}, ST_PUB[NST] = {240, 312, 342, 378}, ST_CHK[NST] = {288, 360, 378, 414};
HD constexpr int st_pub(int g) { for (int i = 0; i < NST; ++i) if (ST_PUB[i] == g) return i; return -1; }
HD constexpr int st_chk(int g) { for (int i = 0; i < NST; ++i) if (ST_CHK[i] == g) return i; return -1; }
struct FSched { int wn[NSLOT + 1], ln[NSLOT + 1], wslot[81], lslot[72]; };
HD constexpr FSched f_make() { FSched F{}; int wn = FW0, ln = K4_X0, cons = 0;
    for (int j = 0; j < FW0; ++j) F.wslot[j] = -2; for (int i = 0; i < K4_X0; ++i) F.lslot[i] = -1;
    for (int g = 0; g < NSLOT; ++g) { F.wn[g] = wn; F.ln[g] = ln;
        if (f_bar(g)) cons = g < 324 ? g : 324 + 2 * (g - 324);
        if ((g < 324 ? (g >= 60 && g % 4 == 2) : g % 3 == 1) && wn < 81 && 8 * wn + 8 <= cons + 144) F.wslot[wn++] = g;
        if (g % 2 == 1) { if (ln < 36) F.lslot[ln++] = g;
            else if (ln < 72 && g > 300) { const int pp = (ln - 36) / 2, cur = g < 324 ? 0 : (g - 324) / 7; int st = 0; while (pp / 2 > ST_TILE[st]) ++st;
                if (pp < cur + K5_AW && g >= ST_CHK[st]) F.lslot[ln++] = g; } } }
    F.wn[NSLOT] = wn; F.ln[NSLOT] = ln; return F; }
constexpr FSched FS = f_make();
HD constexpr int f_after(int slot, int g) { return (FS.wn[g] - FS.wn[slot + 1]) + (FS.ln[g] - FS.ln[slot + 1]); }
HD constexpr int k4_epi_start(int e) { return e == 0 ? 86 : e == 8 ? 327 : 108 + 31 * (e - 1); }
HD constexpr int k4_epi_len(int e) { return e == 0 ? 22 : e == 7 ? 30 : e == 8 ? 18 : 31; }
HD constexpr int k4_epi_first(int e, int gap) { const int g = gap - 2 * k4_epi_start(e), n = 2 * k4_epi_len(e); return g <= 0 ? 0 : g >= n ? 70 : (g * 70 + n - 1) / n; }
HD constexpr int f_barwait(int g) {
    int nb = g + 1; while (nb < NSLOT && !f_bar(nb)) ++nb;
    const int lim = nb < 324 ? nb + FPF : 324 + 2 * (nb - 324 + FPF); int need = (lim + 7) / 8 - 1; if (need > 80) need = 80;
    if (need >= FS.wn[g]) return -1;
    int n = (FS.wn[g] - need - 1) + (FS.ln[g] - (need < FW0 ? 0 : FS.ln[FS.wslot[need]]));
    const int e = st_pub(g) >= 0 ? ST_TILE[st_pub(g)] : -1;
    if (e >= 0) { const int m = f_after(k4_epi_start(e) + k4_epi_len(e) - 1, g); if (m < n) n = m; }
    return n > 63 ? 63 : n; }

DI void phase45(char* ws, char* out, const WStream& S, int w, int qb, int fs, int m128, int j, int s4, unsigned tlo, unsigned thi) {
    const int lane = lane_id(), tt = qb * 8 + w, tq = w & 3, kg = w >> 2;
    const unsigned lv = lane * 16;
    unsigned sq[8]; u32x4 xf[36], af[18][2];
    const char* xs = ws + WS_HC + tt * 36 * 1024;
    const char* as = ws + WS_A + ((m128 * 8 + tq * 2 + kg) * 144 + s4 * 36 + kg) * 1024; const char* as1 = as + (1 - 2 * kg) * 147456;
    auto ldr = [&](auto K) { constexpr int k = K;
        if constexpr (k < 36) gl16c<((k >> 1) % 3) * 1024>(xf[k], lv, xs + ((k & 1) * 18 + (k >> 1) / 3 * 3) * 1024);
        else { constexpr int i = k - 36, p = i / 2; gl16c<0>(af[p][i & 1], lv, ((i & 1) ? as1 : as) + p * 2048); } };
    float A1[2], A3[2], IR2[2];
    unsigned adst = WS_A + ((tt * 2 + (lane >> 5)) * 144) * 1024 + (lane & 31) * 16;
    char* abase = ws + ((fs >> 3) * 36 + ((fs >> 1) & 3)) * 1024 + (fs & 1) * 512;
    unsigned la = lv, lb = lv + 49152, lc = lv + 98304; PIN(la); PIN(lb); PIN(lc);
    unsigned ld, le, lf; u32x2 fv;
    f32x4 acc4[3][4], acc[9][2]; u32x4 wf[FPF]; unsigned pk[4]; float ez[2], ew[2];
    auto rdw = [&](auto G) { constexpr int g = G;
        if constexpr (g < 324) { constexpr int s = g % 144; wf[g % FPF] = lds128((s < 48 ? la : s < 96 ? lb : lc) + (s % 48) * 1024); }
        else { constexpr int s = (36 + 2 * (g - 324)) % 144; wf[g % FPF] = lds128((s < 48 ? ld : s < 96 ? le : lf) + (s % 48) * 1024); } };
    auto epi = [&](auto E, auto I) { constexpr int e = E, i = I, pr = i / 17, st = i % 17, b = pr >> 1, h = st & 1, c = (pr & 1) * 2 + h; f32x4 (&a)[4] = acc4[e % 3];
        if constexpr (i < 68) {
            if constexpr (st < 2) ez[h] = a[b][c] * a[b][c];
            else if constexpr (st < 4) ez[h] = fmaf(ez[h], A3[b], A1[b]);
            else if constexpr (st < 6) ez[h] *= a[b][c];
            else if constexpr (st < 8) ez[h] = __builtin_amdgcn_exp2f(ez[h]);
            else if constexpr (st < 10) ew[h] = a[b][c] * a[2 + b][c];
            else if constexpr (st < 12) ez[h] = fmaf(ez[h], IR2[b], IR2[b]);
            else if constexpr (st < 14) ez[h] = __builtin_amdgcn_rcpf(ez[h]);
            else if constexpr (st < 16) ew[h] *= ez[h];
            else pk[pr] = cvtpk(ew[0], ew[1]);
            if constexpr (st == 16) PIN(pk[pr]); else if constexpr (st == 8 || st == 9 || st >= 14) PIN(ew[h]); else PIN(ez[h]);
        } else if constexpr (i == 68) { const auto r0 = __builtin_amdgcn_permlane32_swap(pk[0], pk[2], false, false); pk[0] = r0[0]; pk[2] = r0[1]; PIN(pk[0]); PIN(pk[2]); }
        else { const auto r1 = __builtin_amdgcn_permlane32_swap(pk[1], pk[3], false, false); gs16c(adst, abase + e * 4096, u32x4{pk[0], r1[0], pk[2], r1[1]}); } };
    unsigned xo, xi, cmu; u32x4 hr; u32x2 h9, hv; f32x4 rv[2]; const int grp = m128 * 4 + j;
    auto gap = [&](auto G) { sfor<9>([&](auto E) { constexpr int f = k4_epi_first(E, G), l = k4_epi_first(E, G + 1); sfor<l - f>([&](auto I) { epi(E, IC<f + I>{}); }); }); SBAR(); };
    sfor<FPF - 1>([&](auto I) { rdw(I); });
    PIN(adst); asm volatile("" : "+s"(xs), "+s"(abase));
    poll_flags(ws, WS_F3 + (blockIdx.x & 7) * 32768 + (qb * 32 + w * 4 + (lane & 3)) * 128, true, tlo, thi);
    sfor<K4_X0>(ldr);
    sfor<NSLOT>([&](auto G) { constexpr int g = G;
        if constexpr (f_bar(g)) {
            if constexpr (st_chk(g) >= 0) { if (w == 1) { asm volatile("s_waitcnt vmcnt(%1)" : "+v"(fv) : "i"(f_after(g - 13, g)) : "memory");
                if (!__all(fv[0] == tlo && fv[1] == thi)) poll_flags(ws, WS_F4 + (qb * 32 + s4 * 8 + (lane & 7)) * 128 + st_chk(g) * 8, true, tlo, thi); } }
            static_assert(f_barwait(g) >= 0, "needed round not issued");
            if constexpr (g == 453) asm volatile("s_waitcnt lgkmcnt(0)" ::: "memory");
            waitv_bar<f_barwait(g)>();
            if constexpr (st_pub(g) >= 0) { if (w == 0 && lane == 0) set_flag(ws, WS_F4 + (qb * 32 + fs) * 128 + st_pub(g) * 8, tlo, thi); }
        } else if constexpr (st_chk(g + 12) >= 0) { if (w == 1) gl8c<st_chk(g + 12) * 8>(fv, WS_F4 + (qb * 32 + s4 * 8 + (lane & 7)) * 128, ws); }
        if constexpr (FS.wn[g + 1] > FS.wn[g]) wround<FS.wn[g]>(S, w);
        if constexpr (FS.ln[g + 1] > FS.ln[g]) ldr(IC<FS.ln[g]>{});
        if constexpr (g >= 30 && g < 46 && g % 2 == 0) gl4c<(g - 30) * 32>(sq[(g - 30) / 2], (lane & 15) * 4, ws + WS_SSQ + tt * 512);
        if constexpr (g == 316) { ld = wd_pos(lane & 15, lane >> 4) + kg * 1024; le = ld + 49152; lf = ld + 98304; PIN(ld); PIN(le); PIN(lf); }
        if constexpr (g < 324) { constexpr int t = k4_tile(g), ks = k4_ks(g), x0 = ks & ~1, m = ks & 1;
            if constexpr (t == 0 && g < 84 && m == 0) { static_assert(FS.ln[g + 1] >= ks + 2, "X not issued");
                constexpr int n = (FS.ln[g + 1] - ks - 2) + (FS.wn[g + 1] - FS.wn[FS.lslot[ks + 1] + 1]);
                asm volatile("s_waitcnt vmcnt(%2)" : "+v"(xf[ks]), "+v"(xf[ks + 1]) : "i"(n) : "memory"); }
            if constexpr (ks < 2) mfma16z(acc4[t % 3][2 * m], wf[g % FPF], xf[x0]); else mfma16a(acc4[t % 3][2 * m], wf[g % FPF], xf[x0]);
            SBAR();
            if constexpr (g == 83) { asm volatile("" : "+v"(sq[0]), "+v"(sq[1]), "+v"(sq[2]), "+v"(sq[3]), "+v"(sq[4]), "+v"(sq[5]), "+v"(sq[6]), "+v"(sq[7]));
                UNR for (int b = 0; b < 2; ++b) {
                    const float var = ((__uint_as_float(sq[b]) + __uint_as_float(sq[2 + b])) + (__uint_as_float(sq[4 + b]) + __uint_as_float(sq[6 + b]))) * (1.f / 576.f) + 1e-5f;
                    const float rs = __builtin_amdgcn_rsqf(var);
                    A1[b] = -2.f * 0.7978845608028654f * 1.4426950408889634f * rs; A3[b] = A1[b] * 0.044715f * rs * rs; IR2[b] = var; PIN(A1[b]); PIN(A3[b]); PIN(IR2[b]); } }
            gap(IC<2 * g>{}); rdw(IC<g + FPF - 1>{}); SBAR();
            if constexpr (ks < 2) mfma16z(acc4[t % 3][2 * m + 1], wf[g % FPF], xf[x0 + 1]); else mfma16a(acc4[t % 3][2 * m + 1], wf[g % FPF], xf[x0 + 1]);
            SBAR();
            gap(IC<2 * g + 1>{});
        } else { constexpr int q = g - 324, p = k5_per(q), T = k5_tile(q);
            if constexpr (T == 0) { constexpr int i = 36 + 2 * p + 1; static_assert(FS.ln[g + 1] > i, "a not issued");
                constexpr int n = (FS.ln[g + 1] - i - 1) + (FS.wn[g + 1] - FS.wn[FS.lslot[i] + 1]);
                asm volatile("s_waitcnt vmcnt(%2)" : "+v"(af[p][0]), "+v"(af[p][1]) : "i"(n) : "memory"); }
            constexpr int xd = q >= 129 && q < 141 ? (q - 129) / 3 : -1, xs3 = (q - 129) % 3;
            if constexpr (p == 0) mfma16z(acc[T][0], wf[g % FPF], af[0][0]); else mfma16a(acc[T][0], wf[g % FPF], af[p][0]);
            SBAR();
            gap(IC<2 * g>{});
            if constexpr (xd >= 0 && xs3 == 1) { UNR for (int e = 0; e < 4; ++e) { acc[2 * xd][0][e] += rv[0][e]; PIN(acc[2 * xd][0]); } }
            if constexpr (xd >= 0 && xd < 3 && xs3 == 2)
                gs16c(lv, ws + WS_XB + (grp * 4 + ((s4 + 1 + xd) & 3)) * 32768 + ((2 - xd) * 8 + w) * 1024, u32x4{cvtpk(acc[2 * xd][0][0], acc[2 * xd + 1][0][0]),
                      cvtpk(acc[2 * xd][0][1], acc[2 * xd + 1][0][1]), cvtpk(acc[2 * xd][0][2], acc[2 * xd + 1][0][2]), cvtpk(acc[2 * xd][0][3], acc[2 * xd + 1][0][3])});
            if constexpr (xd == 3 && xs3 == 2) {
                if (tq != s4) gs8c(lane * 8, ws + WS_XB + (grp * 4 + tq) * 32768 + 24576 + (((s4 - tq - 1) & 3) * 2 + kg) * 512, u32x2{cvtpk(acc[6][0][0], acc[6][0][1]), cvtpk(acc[6][0][2], acc[6][0][3])}); }
            if constexpr (g + FPF - 1 < NSLOT) rdw(IC<g + FPF - 1>{});
            SBAR();
            if constexpr (p == 0) mfma16z(acc[T][1], wf[g % FPF], af[0][1]); else mfma16a(acc[T][1], wf[g % FPF], af[p][1]);
            SBAR();
            gap(IC<2 * g + 1>{});
            if constexpr (xd >= 0 && xd < 3 && xs3 == 1) { UNR for (int e = 0; e < 4; ++e) { acc[2 * xd + 1][0][e] += rv[1][e]; PIN(acc[2 * xd + 1][0]); } }
            if constexpr (xd >= 0 && xs3 == 0) { rv[0] = *(const LAS f32x4*)(xi + 2 * xd * 1024); if constexpr (xd < 3) rv[1] = *(const LAS f32x4*)(xi + (2 * xd + 1) * 1024); }
            if constexpr (q == 118) { xo = 73728 + w * 7168 + lv; xi = 73728 + (w ^ 4) * 7168 + lv; PIN(xo); PIN(xi); }
            if constexpr (q >= 122 && q < 129) *(LAS f32x4*)(xo + (q - 122) * 1024) = acc[q - 122][1];
            if constexpr (q == 142) {
                const char* hs = ws + WS_HC + (m128 * 8 + tq * 2 + kg) * 18432;
                gl16c<0>(hr, lv, hs + j * 4096 + s4 * 1024); gl8c<0>(h9, (lane >> 5) * 256 + (lane & 15) * 16 + ((lane >> 4) & 1) * 8, hs + 16384 + j * 512); gl4c<0>(cmu, (m128 * 128 + tq * 32 + kg * 16 + (lane & 15)) * 4, ws + WS_CM); }
            if constexpr (q == 148) { waitv<3>(); if (lane < 3) set_flag(ws, WS_XB + (grp * 4 + ((s4 + 1 + lane) & 3)) * 32768 + 27648 + ((2 - lane) * 8 + w) * 128, tlo, thi); }
            if constexpr (q == 156) gl8c<0>(hv, WS_XB + (grp * 4 + s4) * 32768 + 27648 + ((lane < 3 ? lane : 0) * 8 + w) * 128, ws);
            SBAR(); }
    });
    const char* src = ws + WS_XB + (grp * 4 + s4) * 32768;
    asm volatile("s_nop 7\n\ts_waitcnt vmcnt(0)" : "+v"(hv), "+v"(acc[7][0]), "+v"(acc[7][1]), "+v"(acc[8][0]), "+v"(acc[8][1]), "+v"(hr), "+v"(h9), "+v"(cmu) :: "memory");
    *(LAS f32x4*)(147456 + w * 2048 + lv) = acc[7][1]; *(LAS f32x4*)(147456 + 1024 + w * 2048 + lv) = acc[8][1];
    if (!__all(hv[0] == tlo && hv[1] == thi)) poll_flags(src, 27648 + ((lane < 3 ? lane : 0) * 8 + w) * 128, true, tlo, thi);
    u32x4 pa[3]; u32x2 pb[3]; const unsigned o9 = tq == s4 ? 24576 + kg * 512 + lane * 8 : w * 1024 + lane * 8, s9 = tq == s4 ? 1024 : 8192;
    auto sweep = [&]() { UNR for (int d = 0; d < 3; ++d) { gl16c<0>(pa[d], lv, src + (d * 8 + w) * 1024); gl8c<0>(pb[d], o9, src + d * s9); } };
    sweep();
    lbar();
    const float cm = __uint_as_float(cmu); const unsigned oo = (m128 * 128 + tq * 32 + kg * 16 + (lane & 15)) * 1152;
    f32x4 v0 = acc[7][0] + *(const LAS f32x4*)(147456 + (w ^ 4) * 2048 + lv), v1 = acc[8][0] + *(const LAS f32x4*)(147456 + 1024 + (w ^ 4) * 2048 + lv), v8 = acc[6][0];
    UNR for (int e = 0; e < 4; ++e) { v0[e] += bflo(hr[e]) + cm; v1[e] += bfhi(hr[e]) + cm; }
    v8 += f32x4{bflo(h9[0]), bfhi(h9[0]), bflo(h9[1]), bfhi(h9[1])} + cm; PIN(v0); PIN(v1); PIN(v8);
    asm volatile("s_waitcnt vmcnt(0)" : "+v"(pa[0]), "+v"(pa[1]), "+v"(pa[2]), "+v"(pb[0]), "+v"(pb[1]), "+v"(pb[2]) :: "memory");
    UNR for (int d = 0; d < 3; ++d) UNR for (int e = 0; e < 4; ++e) { v0[e] += bflo(pa[d][e]); v1[e] += bfhi(pa[d][e]); }
    gs16c(oo + j * 256 + s4 * 64 + (lane >> 4) * 16, out, u32x4{cvtpk(v0[0], v1[0]), cvtpk(v0[1], v1[1]), cvtpk(v0[2], v1[2]), cvtpk(v0[3], v1[3])});
    if (tq == s4) { UNR for (int d = 0; d < 3; ++d) v8 += f32x4{bflo(pb[d][0]), bfhi(pb[d][0]), bflo(pb[d][1]), bfhi(pb[d][1])};
        *(GAS u32x2*)((GAS char*)out + (oo + 1024 + j * 32 + (lane >> 4) * 8)) = u32x2{cvtpk(v8[0], v8[1]), cvtpk(v8[2], v8[3])}; }
}

__global__ __launch_bounds__(512, 2) void block_fwd(const char* __restrict__ x, const char* __restrict__ wqkv, const char* __restrict__ wo, const char* __restrict__ wg,
                                                    const char* __restrict__ wu, const char* __restrict__ wd, char* __restrict__ ws, char* __restrict__ out) {
    extern __shared__ __attribute__((aligned(16))) char smem[];
    const int w = rfl(threadIdx.x >> 6), xc = blockIdx.x & 7, rk = blockIdx.x >> 3, hs = rk & 3, qb = rk >> 2;
    asm volatile("" : "+s"(x), "+s"(wqkv), "+s"(wo), "+s"(wg), "+s"(wu), "+s"(wd), "+s"(ws), "+s"(out));
    u32x2 ep; gl8c<0>(ep, WS_EPOCH, ws);
    const char* pc0; unsigned clen;
    asm volatile("s_getpc_b64 %0\n.Lblk_begin:\n\ts_mov_b32 %1, .Lblk_end-.Lblk_begin" : "=s"(pc0), "=s"(clen));
    auto prefetch = [&](int l, bool late) { const int pp = xc * 2 + (hs >> 1);
        if (late) { if (w == 1) touch(WS_F1 + xc * 32768 + (((l < 32 ? 16 : 32) + pp) / 6 * 32 + (l & 31)) * 128, ws, 163072); if (w == 2) touch(WS_F1 + xc * 32768 + (pp / 6 * 32 + qb * 4 + (l & 3)) * 128, ws, 162816); }
        else if (w == 7) { const unsigned o = (rk + (l & 31) * 32) * 128; touch(o < clen ? o : 0u, pc0, 163328); } };
    auto prefetch2 = [&](int l) {
        if (w == 7) { const unsigned o = (rk + (l & 31) * 32) * 128; touch(o < clen ? o : 0u, pc0, 163328); }
        if (w == 5) touch(l < 32 ? WS_XB + (((qb * 2 + (xc & 1)) * 4 + hs) * 4 + (xc >> 1)) * 32768 + 27648 + l * 128 : WS_F4 + (qb * 32 + l - 32) * 128, ws, 162816);
        if (w == 6) touch(l < 32 ? WS_F2 + ((qb * 8 + xc) * 32 + l) * 128 : WS_F3 + xc * 32768 + (qb * 32 + l - 32) * 128, ws, 163072); };
    const int mt1 = (xc >> 1) * 8 + (rk & 7), ct1 = (xc & 1) * 4 + (rk >> 3);
    phase1(prefetch, x, wqkv, wo, ws, w, mt1, ct1);
    RtTask RT; rt_setup(RT, wg, wu, wd, blockIdx.x * 8 + w);
    unsigned po1, po2; { const int lane = lane_id(), pp = xc * 2 + (hs >> 1);
        po1 = WS_F1 + xc * 32768 + (((lane < 32 ? 16 : 32) + pp) / 6 * 32 + (lane & 31)) * 128; po2 = WS_F1 + xc * 32768 + (pp / 6 * 32 + qb * 4 + (lane & 3)) * 128; PIN(po1); PIN(po2); }
    asm volatile("s_waitcnt vmcnt(0) lgkmcnt(0)\n\ts_barrier" : "+v"(ep) :: "memory");
    const u64 tag = (((u64)rfl(ep[1]) << 32) | rfl(ep[0])) + 1; const unsigned tlo = (unsigned)tag, thi = (unsigned)(tag >> 32);
    if (w == 0) { const int lane = lane_id(); if (lane < 9) set_flag(ws, lane == 8 ? WS_FD + blockIdx.x * 8 : WS_F1 + lane * 32768 + (ct1 * 32 + mt1) * 128, tlo, thi); }
    P3Pre P;
    const int fs4 = xc * 4 + hs, m5 = qb * 2 + (xc & 1), j5 = hs, s5 = xc >> 1;
    phase2(prefetch2, x, RT, ws, P, po1, po2, w, xc * 4 + hs, qb, qb * 8 + xc, hs, tlo, thi);
    lbar();
    UNR for (int i = 0; i < 9; ++i) asm volatile("" : "+v"(P.wf[i][0]), "+v"(P.wf[i][1]), "+v"(P.wf[i][2]));
    asm volatile("" : "+v"(P.xa), "+v"(P.xb), "+v"(P.xm));
    WStream S; S.lv = lane_id() * 16; S.lw = w * 1024;
    S.a = ws + WS_WGU + fs4 * 324 * 1024 + w * 1024;
    S.b = ws + WS_WD + (j5 * 144 + s5 * 36) * 9216 + w * 1024 - 4096;
    phase3(ws, P, S, w, qb * 8 + xc, hs, qb, tlo, thi);
    fullbar();
    if (w == 0) { const int lane = lane_id(); if (lane < 8) set_flag(ws, WS_F3 + lane * 32768 + (qb * 32 + xc * 4 + hs) * 128, tlo, thi); }
    sfor<FW0 - 10>([&](auto J) { wround<10 + J>(S, w); });
    phase45(ws, out, S, w, qb, fs4, m5, j5, s5, tlo, thi);
    asm volatile(".Lblk_end:");
    if (blockIdx.x == 0 && w == 0 && lane_id() == 0) set_flag(ws, WS_EPOCH, tlo, thi);
}

extern "C" void kernel_launch(void* const* d_in, const int* in_sizes, int n_in, void* d_out, int out_size, void* d_ws, size_t ws_size, hipStream_t stream) {
    (void)hipFuncSetAttribute((const void*)block_fwd, hipFuncAttributeMaxDynamicSharedMemorySize, LDS_BYTES);
    block_fwd<<<256, 512, LDS_BYTES, stream>>>((const char*)d_in[0], (const char*)d_in[1], (const char*)d_in[2], (const char*)d_in[4], (const char*)d_in[5], (const char*)d_in[6],
                                                (char*)d_ws, (char*)d_out);
}
```
